```python
import torch

def _softmax(x, axis=-1):
    return torch.softmax(x, dim=axis)
def _gelu(x):
    return torch.nn.functional.gelu(x, approximate="tanh")
def _silu(x):
    return torch.nn.functional.silu(x)
def _sigmoid(x):
    return torch.sigmoid(x)
def _layernorm(x, axis=-1, eps=1e-5):
    m = x.mean(dim=axis, keepdim=True)
    v = x.var(dim=axis, keepdim=True, unbiased=False)
    return (x - m) / torch.sqrt(v + eps)
def _scatter_add(tgt, idx, src, axis=0):
    return tgt.clone().index_add_(axis, idx, src)
def _segment_sum(src, seg, n):
    out = torch.zeros((n,) + src.shape[1:], dtype=src.dtype)
    return out.index_add_(0, seg, src)
def _pad(x, pw):
    flat = tuple(v for pair in reversed(pw) for v in pair)
    return torch.nn.functional.pad(x, flat)

kernel_name = 'standalone__weight_only_int_quip_2__prompt'

def setup_inputs():

    _w = (lambda a: (lambda t: t.to(torch.bfloat16) if t.is_floating_point() else t)(torch.as_tensor(a)))
    B, S, D, F = 1, 2048, 576, 4608
    nbits = max(2, min(4, int(4)))
    G = 64 if 64 > 0 else D
    D = (D // G) * G
    ng = D // G
    qmax = 1 << nbits
    qw = torch.randint(0, qmax, (D, F)).to(torch.int32)
    zp = torch.randint(0, qmax, (ng, F)).to(float)
    sc = torch.abs((lambda *s: torch.randn(*s, dtype=torch.bfloat16) * 0.05)(ng, F)).to(float) * 0.1 + 0.02
    return {
        "x": _w((lambda *s: torch.randn(*s, dtype=torch.bfloat16) * 0.05)(B, S, D)),
        "qw": _w(qw),
        "scales": _w(sc),
        "zeros": _w(zp),
    }

def reference(x, qw, scales, zeros):
    B, S, D = x.shape
    ng, F = scales.shape
    G = D // ng
    s = (lambda x, n, axis: torch.repeat_interleave(x, n, dim=axis))(scales, G, axis=0)
    z = (lambda x, n, axis: torch.repeat_interleave(x, n, dim=axis))(zeros, G, axis=0)
    w = ((lambda x, dt: x.to(dt))(qw, x.dtype) - z) * s
    return torch.einsum("bsd,df->bsf", x, w)
```

```cpp
#include <hip/hip_runtime.h>
#include <hip/hip_bf16.h>
#include <stdint.h>

__device__ __forceinline__ float bf2f(unsigned short v){ return __uint_as_float(((unsigned)v)<<16); }
__device__ __forceinline__ float rbf(float f){ return __bfloat162float(__float2bfloat16(f)); }

__global__ void naive_kernel(const unsigned short* __restrict__ x, const int* __restrict__ qw,
                             const unsigned short* __restrict__ sc, const unsigned short* __restrict__ zp,
                             __hip_bfloat16* __restrict__ out, int M, int K, int N, int G) {
    int n = blockIdx.x * 256 + threadIdx.x;
    int m0 = blockIdx.y * 8;
    float acc[8];
    for (int i = 0; i < 8; i++) acc[i] = 0.f;
    for (int k = 0; k < K; k++) {
        int g = k / G;
        float s = bf2f(sc[g * N + n]);
        float z = bf2f(zp[g * N + n]);
        float q = rbf((float)qw[(size_t)k * N + n]);
        float w = rbf(rbf(q - z) * s);
        for (int i = 0; i < 8; i++) acc[i] += bf2f(x[(size_t)(m0 + i) * K + k]) * w;
    }
    for (int i = 0; i < 8; i++) out[(size_t)(m0 + i) * N + n] = __float2bfloat16(acc[i]);
}

extern "C" void kernel_launch(void* const* d_in, const int* in_sizes, int n_in,
                              void* d_out, int out_size, void* d_ws, size_t ws_size,
                              hipStream_t stream) {
    const int M = 2048, K = 576, N = 4608, G = 64;
    naive_kernel<<<dim3(N / 256, M / 8), 256, 0, stream>>>((const unsigned short*)d_in[0], (const int*)d_in[1],
        (const unsigned short*)d_in[2], (const unsigned short*)d_in[3], (__hip_bfloat16*)d_out, M, K, N, G);
}
```

```cpp
#include <hip/hip_runtime.h>
#include <hip/hip_bf16.h>
#include <stdint.h>

constexpr int M_ = 2048, K_ = 576, N_ = 4608;
constexpr int LDS_BYTES = 152640;

__global__ __launch_bounds__(512, 2)
void wq_gemm(const unsigned short* __restrict__ x, const int* __restrict__ qw,
             const unsigned short* __restrict__ sc, const unsigned short* __restrict__ zp,
             unsigned short* __restrict__ out) {
    const unsigned lane = threadIdx.x & 63;
    const unsigned wave = __builtin_amdgcn_readfirstlane(threadIdx.x >> 6);
    const unsigned wg = blockIdx.x;
    const unsigned xcd = wg & 7, idx = wg >> 3;
    const unsigned tile = xcd < 4 ? xcd * 32 + idx : 128 + (xcd - 4) * 31 + idx;
    const unsigned cg = tile / 7, rg = tile - cg * 7;
    const unsigned blk0 = rg * 18 + (rg < 2 ? rg : 2);
    const unsigned nblk = rg < 2 ? 19 : 18;
    const unsigned cgb = cg * 128;
    const unsigned rot = (rg + 3 * (cg % 3)) % 9;
    const unsigned start = (cg % 5) * 4;
    __amdgpu_buffer_rsrc_t rx = __builtin_amdgcn_make_buffer_rsrc((void*)x, 0, M_ * K_ * 2, 0x00020000);
    __amdgpu_buffer_rsrc_t rq = __builtin_amdgcn_make_buffer_rsrc((void*)qw, 0, K_ * N_ * 4, 0x00020000);
    __amdgpu_buffer_rsrc_t rs = __builtin_amdgcn_make_buffer_rsrc((void*)sc, 0, 9 * N_ * 2, 0x00020000);
    __amdgpu_buffer_rsrc_t rz = __builtin_amdgcn_make_buffer_rsrc((void*)zp, 0, 9 * N_ * 2, 0x00020000);
    __amdgpu_buffer_rsrc_t ro = __builtin_amdgcn_make_buffer_rsrc((void*)out, 0, M_ * N_ * 2, 0x00020000);
    asm volatile(
        "L_begin_%=:\n\t"
        "s_getpc_b64 s[64:65]\n\t"
        "L_pcref_%=:\n\t"
        "s_sub_u32 s64, s64, L_pcref_%=-L_begin_%=\n\t"
        "s_subb_u32 s65, s65, 0\n\t"
        "s_lshr_b32 s40, %[wg], 3\n\t"
        "s_and_b32 s40, s40, 31\n\t"
        "s_cmp_lg_u32 %[wave], 7\n\t"
        "s_cbranch_scc1 L_nopfc_%=\n\t"
        "s_min_u32 s40, s40, (L_hot_end_%=-L_begin_%=)/1024\n\t"
        "s_lshl_b32 s40, s40, 10\n\t"
        "v_lshl_add_u32 v125, %[lane], 4, s40\n\t"
        "global_load_dwordx4 a[120:123], v125, s[64:65]\n\t"
        "L_nopfc_%=:\n\t"
        "v_lshlrev_b32 v48, 2, %[lane]\n\t"
        "s_mul_i32 s40, %[wave], 9216\n\t"
        "s_lshl_b32 s41, %[cgb], 1\n\t"
        "s_add_u32 s40, s40, s41\n\t"
        "buffer_load_dword v16, v48, %[rs], s40 offen\n\t"
        "buffer_load_dword v17, v48, %[rz], s40 offen\n\t"
        "s_add_u32 s40, s41, 73728\n\t"
        "s_cmp_eq_u32 %[wave], 0\n\t"
        "s_cselect_b32 s40, s40, 0x7f000000\n\t"
        "buffer_load_dword v18, v48, %[rs], s40 offen\n\t"
        "buffer_load_dword v19, v48, %[rz], s40 offen\n\t"
        "s_mov_b32 s54, %[rot]\n\t"
        "v_bfe_u32 v124, %[lane], 3, 1\n\t"
        "v_lshrrev_b32 v123, 4, %[lane]\n\t"
        "v_and_b32 v108, 7, %[lane]\n\t"
        "v_lshl_or_b32 v123, v123, 3, v108\n\t"
        "s_mul_i32 s40, %[wave], 147456\n\t"
        "s_lshl_b32 s41, %[cgb], 2\n\t"
        "v_mul_u32_u24 v108, 73728, v124\n\t"
        "s_add_u32 s40, s40, s41\n\t"
        "v_lshl_add_u32 v108, v123, 4, v108\n\t"
        "v_add_u32 v108, s40, v108\n\t"
        "s_mul_i32 s52, s54, 1179648\n\t"
        "buffer_load_dwordx4 v[60:63], v108, %[rq], s52 offen\n\t"
        "s_add_u32 s52, s52, 18432\n\t"
        "buffer_load_dwordx4 v[64:67], v108, %[rq], s52 offen\n\t"
        "s_add_u32 s52, s52, 18432\n\t"
        "buffer_load_dwordx4 v[68:71], v108, %[rq], s52 offen\n\t"
        "s_add_u32 s52, s52, 18432\n\t"
        "buffer_load_dwordx4 v[72:75], v108, %[rq], s52 offen\n\t"
        "v_lshrrev_b32 v48, 3, %[lane]\n\t"
        "v_and_b32 v49, 6, v48\n\t"
        "v_and_b32 v50, 7, %[lane]\n\t"
        "v_xor_b32 v49, v49, v50\n\t"
        "v_mul_u32_u24 v48, 1152, v48\n\t"
        "v_lshl_add_u32 v48, v49, 4, v48\n\t"
        "s_and_b32 s42, %[wave], 1\n\t"
        "s_mul_i32 s42, s42, 9216\n\t"
        "v_add_u32 v48, s42, v48\n\t"
        "v_accvgpr_write_b32 a127, v48\n\t"
        "s_lshr_b32 s41, %[wave], 1\n\t"
        "s_lshl_b32 s47, %[wave], 10\n\t"
        "s_add_u32 s40, s41, 0\n\t"
        "s_add_u32 s46, %[start], s40\n\t"
        "s_cmp_ge_u32 s46, %[nblk]\n\t"
        "s_cselect_b32 s43, %[nblk], 0\n\t"
        "s_sub_u32 s46, s46, s43\n\t"
        "s_add_u32 s46, s46, %[blk0]\n\t"
        "s_mul_i32 s46, s46, 18432\n\t"
        "v_add_u32 v109, s46, v48\n\t"
        "s_add_u32 s40, s41, 4\n\t"
        "s_add_u32 s46, %[start], s40\n\t"
        "s_cmp_ge_u32 s46, %[nblk]\n\t"
        "s_cselect_b32 s43, %[nblk], 0\n\t"
        "s_sub_u32 s46, s46, s43\n\t"
        "s_add_u32 s46, s46, %[blk0]\n\t"
        "s_mul_i32 s46, s46, 18432\n\t"
        "v_add_u32 v110, s46, v48\n\t"
        "s_add_u32 m0, s47, 0\n\t"
        "s_lshl_b32 s40, s54, 7\n\t"
        "buffer_load_dwordx4 v109, %[rx], s40 offen lds\n\t"
        "s_add_u32 m0, s47, 8192\n\t"
        "s_lshl_b32 s40, s54, 7\n\t"
        "buffer_load_dwordx4 v110, %[rx], s40 offen lds\n\t"
        "s_add_u32 s55, s54, 1\n\t"
        "s_cmp_ge_u32 s55, 9\n\t"
        "s_cselect_b32 s55, 0, s55\n\t"
        "s_add_u32 s56, s55, 1\n\t"
        "s_cmp_ge_u32 s56, 9\n\t"
        "s_cselect_b32 s56, 0, s56\n\t"
        "s_add_u32 s57, s56, 1\n\t"
        "s_cmp_ge_u32 s57, 9\n\t"
        "s_cselect_b32 s57, 0, s57\n\t"
        "s_add_u32 s58, s57, 1\n\t"
        "s_cmp_ge_u32 s58, 9\n\t"
        "s_cselect_b32 s58, 0, s58\n\t"
        "s_add_u32 s59, s58, 1\n\t"
        "s_cmp_ge_u32 s59, 9\n\t"
        "s_cselect_b32 s59, 0, s59\n\t"
        "s_add_u32 s60, s59, 1\n\t"
        "s_cmp_ge_u32 s60, 9\n\t"
        "s_cselect_b32 s60, 0, s60\n\t"
        "s_add_u32 s61, s60, 1\n\t"
        "s_cmp_ge_u32 s61, 9\n\t"
        "s_cselect_b32 s61, 0, s61\n\t"
        "s_add_u32 s62, s61, 1\n\t"
        "s_cmp_ge_u32 s62, 9\n\t"
        "s_cselect_b32 s62, 0, s62\n\t"
        "s_and_b32 s44, %[wave], 3\n\t"
        "s_lshr_b32 s45, %[wave], 2\n\t"
        "v_lshrrev_b32 v48, 3, v123\n\t"
        "v_lshlrev_b32 v111, 11, v48\n\t"
        "v_and_b32 v48, 1, v123\n\t"
        "v_lshl_add_u32 v111, v48, 10, v111\n\t"
        "v_lshl_add_u32 v111, v48, 5, v111\n\t"
        "v_bfe_u32 v48, v123, 1, 2\n\t"
        "v_lshl_add_u32 v111, v48, 6, v111\n\t"
        "v_lshrrev_b32 v48, 1, v48\n\t"
        "v_lshl_add_u32 v111, v48, 4, v111\n\t"
        "v_lshl_add_u32 v111, v124, 3, v111\n\t"
        "s_lshl_b32 s40, s45, 13\n\t"
        "s_lshl_b32 s41, s44, 8\n\t"
        "s_add_u32 s40, s40, s41\n\t"
        "s_add_u32 s40, s40, 110592\n\t"
        "v_add_u32 v111, s40, v111\n\t"
        "v_xor_b32 v112, 16, v111\n\t"
        "v_xor_b32 v113, 32, v111\n\t"
        "v_xor_b32 v114, 48, v111\n\t"
        "v_lshlrev_b32 v121, 4, v123\n\t"
        "v_add_u32 v121, 143360, v121\n\t"
        "v_and_b32 v48, 15, %[lane]\n\t"
        "v_lshrrev_b32 v49, 4, %[lane]\n\t"
        "v_lshrrev_b32 v50, 3, v48\n\t"
        "v_lshlrev_b32 v115, 10, v50\n\t"
        "v_and_b32 v50, 7, v48\n\t"
        "v_lshl_add_u32 v115, v50, 7, v115\n\t"
        "v_and_b32 v50, 6, v48\n\t"
        "v_xor_b32 v50, v50, v49\n\t"
        "v_lshl_add_u32 v115, v50, 4, v115\n\t"
        "s_lshl_b32 s40, s45, 13\n\t"
        "v_add_u32 v115, s40, v115\n\t"
        "v_xor_b32 v116, 64, v115\n\t"
        "v_add_u32 v117, 55296, v115\n\t"
        "v_add_u32 v118, 55296, v116\n\t"
        "v_lshrrev_b32 v50, 3, v48\n\t"
        "v_xor_b32 v50, v50, v48\n\t"
        "v_lshl_add_u32 v50, v49, 4, v50\n\t"
        "v_lshlrev_b32 v50, 4, v50\n\t"
        "s_lshl_b32 s40, s44, 11\n\t"
        "s_add_u32 s40, s40, 110592\n\t"
        "v_add_u32 v119, s40, v50\n\t"
        "v_xor_b32 v120, 32, v119\n\t"
        "v_add_u32 v120, 0x400, v120\n\t"
        "s_mov_b32 s48, 0x4b800000\n\t"
        "s_mov_b32 s49, 0x43000000\n\t"
        "s_waitcnt vmcnt(6)\n\t"
        "v_mov_b32 v48, 0\n\t"
        "v_lshlrev_b32 v49, 3, %[lane]\n\t"
        "s_lshl_b32 s40, %[wave], 9\n\t"
        "s_add_u32 s40, s40, 143360\n\t"
        "v_add_u32 v49, s40, v49\n\t"
        "v_lshlrev_b32 v92, 16, v16\n\t"
        "v_and_b32 v93, 0xffff0000, v16\n\t"
        "v_lshlrev_b32 v94, 16, v17\n\t"
        "v_and_b32 v95, 0xffff0000, v17\n\t"
        "v_mul_f32 v96, s48, v92\n\t"
        "v_mul_f32_e64 v98, -v92, v94\n\t"
        "v_mul_f32 v97, s48, v93\n\t"
        "v_mul_f32_e64 v99, -v93, v95\n\t"
        "ds_write_b64 v49, v[96:97] offset:0\n\t"
        "ds_write_b64 v49, v[98:99] offset:4608\n\t"
        "v_rndne_f32 v50, v94\n\t"
        "v_max_f32_e64 v51, |v94|, s49\n\t"
        "v_sub_f32 v50, v94, v50\n\t"
        "v_subrev_f32 v51, s49, v51\n\t"
        "v_or3_b32 v48, v48, v50, v51\n\t"
        "v_bfe_u32 v50, v92, 23, 8\n\t"
        "v_subrev_u32 v50, 27, v50\n\t"
        "v_max_u32 v50, 0xc8, v50\n\t"
        "v_xor_b32 v50, 0xc8, v50\n\t"
        "v_or_b32 v48, v48, v50\n\t"
        "v_rndne_f32 v50, v95\n\t"
        "v_max_f32_e64 v51, |v95|, s49\n\t"
        "v_sub_f32 v50, v95, v50\n\t"
        "v_subrev_f32 v51, s49, v51\n\t"
        "v_or3_b32 v48, v48, v50, v51\n\t"
        "v_bfe_u32 v50, v93, 23, 8\n\t"
        "v_subrev_u32 v50, 27, v50\n\t"
        "v_max_u32 v50, 0xc8, v50\n\t"
        "v_xor_b32 v50, 0xc8, v50\n\t"
        "v_or_b32 v48, v48, v50\n\t"
        "s_cmp_lg_u32 %[wave], 0\n\t"
        "s_cbranch_scc1 L_tabdone_%=\n\t"
        "v_lshlrev_b32 v92, 16, v18\n\t"
        "v_and_b32 v93, 0xffff0000, v18\n\t"
        "v_lshlrev_b32 v94, 16, v19\n\t"
        "v_and_b32 v95, 0xffff0000, v19\n\t"
        "v_mul_f32 v96, s48, v92\n\t"
        "v_mul_f32_e64 v98, -v92, v94\n\t"
        "v_mul_f32 v97, s48, v93\n\t"
        "v_mul_f32_e64 v99, -v93, v95\n\t"
        "ds_write_b64 v49, v[96:97] offset:4096\n\t"
        "ds_write_b64 v49, v[98:99] offset:8704\n\t"
        "v_rndne_f32 v50, v94\n\t"
        "v_max_f32_e64 v51, |v94|, s49\n\t"
        "v_sub_f32 v50, v94, v50\n\t"
        "v_subrev_f32 v51, s49, v51\n\t"
        "v_or3_b32 v48, v48, v50, v51\n\t"
        "v_bfe_u32 v50, v92, 23, 8\n\t"
        "v_subrev_u32 v50, 27, v50\n\t"
        "v_max_u32 v50, 0xc8, v50\n\t"
        "v_xor_b32 v50, 0xc8, v50\n\t"
        "v_or_b32 v48, v48, v50\n\t"
        "v_rndne_f32 v50, v95\n\t"
        "v_max_f32_e64 v51, |v95|, s49\n\t"
        "v_sub_f32 v50, v95, v50\n\t"
        "v_subrev_f32 v51, s49, v51\n\t"
        "v_or3_b32 v48, v48, v50, v51\n\t"
        "v_bfe_u32 v50, v93, 23, 8\n\t"
        "v_subrev_u32 v50, 27, v50\n\t"
        "v_max_u32 v50, 0xc8, v50\n\t"
        "v_xor_b32 v50, 0xc8, v50\n\t"
        "v_or_b32 v48, v48, v50\n\t"
        "L_tabdone_%=:\n\t"
        "v_cmp_ne_u32 vcc, 0, v48\n\t"
        "s_lshl_b32 s41, %[wave], 2\n\t"
        "s_add_u32 s41, s41, 152576\n\t"
        "v_mov_b32 v93, s41\n\t"
        "s_nop 2\n\t"
        "s_or_b32 s40, vcc_lo, vcc_hi\n\t"
        "s_cselect_b32 s40, -1, 0\n\t"
        "v_mov_b32 v92, s40\n\t"
        "ds_write_b32 v93, v92\n\t"
        "v_mov_b32 v124, 152576\n\t"
        "s_waitcnt lgkmcnt(0)\n\t"
        "s_barrier\n\t"
        "ds_read_b128 v[48:51], v124\n\t"
        "ds_read_b128 v[52:55], v124 offset:16\n\t"
        "s_lshl_b32 s40, s54, 9\n\t"
        "v_add_u32 v124, s40, v121\n\t"
        "ds_read_b128 v[100:103], v124\n\t"
        "ds_read_b128 v[104:107], v124 offset:4608\n\t"
        "s_mul_i32 s52, s55, 1179648\n\t"
        "buffer_load_dwordx4 v[76:79], v108, %[rq], s52 offen\n\t"
        "s_add_u32 s52, s52, 18432\n\t"
        "buffer_load_dwordx4 v[80:83], v108, %[rq], s52 offen\n\t"
        "s_add_u32 s52, s52, 18432\n\t"
        "buffer_load_dwordx4 v[84:87], v108, %[rq], s52 offen\n\t"
        "s_add_u32 s52, s52, 18432\n\t"
        "buffer_load_dwordx4 v[88:91], v108, %[rq], s52 offen\n\t"
        "s_add_u32 m0, s47, 18432\n\t"
        "s_lshl_b32 s40, s55, 7\n\t"
        "buffer_load_dwordx4 v109, %[rx], s40 offen lds\n\t"
        "s_add_u32 m0, s47, 26624\n\t"
        "s_lshl_b32 s40, s55, 7\n\t"
        "buffer_load_dwordx4 v110, %[rx], s40 offen lds\n\t"
        "s_waitcnt lgkmcnt(2)\n\t"
        "v_or3_b32 v122, v48, v49, v50\n\t"
        "v_or3_b32 v122, v122, v51, v52\n\t"
        "v_or3_b32 v122, v122, v53, v54\n\t"
        "v_or_b32 v122, v122, v55\n\t"
        "s_and_b32 s40, s44, 1\n\t"
        "s_lshl_b32 s40, s40, 10\n\t"
        "s_lshr_b32 s41, s44, 1\n\t"
        "s_lshl_b32 s41, s41, 11\n\t"
        "s_add_u32 s53, s40, s41\n\t"
        "; interval 0\n\t"
        "s_waitcnt vmcnt(8)\n\t"
        "v_or3_b32 v123, v122, v60, v61\n\t"
        "v_or3_b32 v124, v62, v63, v64\n\t"
        "v_or3_b32 v123, v123, v65, v66\n\t"
        "v_or3_b32 v124, v124, v67, v68\n\t"
        "v_or3_b32 v123, v123, v69, v70\n\t"
        "v_or3_b32 v124, v124, v71, v72\n\t"
        "v_or3_b32 v123, v123, v73, v74\n\t"
        "v_or3_b32 v123, v123, v124, v75\n\t"
        "v_cmp_lt_u32 vcc, 0x7f, v123\n\t"
        "s_waitcnt lgkmcnt(0)\n\t"
        "s_cbranch_vccnz L_slow0_%=\n\t"
        "s_mul_i32 s52, s56, 1179648\n\t"
        "v_fma_mix_f32 v60, v60, v100, v104 op_sel_hi:[1,0,0]\n\t"
        "v_fma_mix_f32 v61, v61, v101, v105 op_sel_hi:[1,0,0]\n\t"
        "v_fma_mix_f32 v62, v62, v102, v106 op_sel_hi:[1,0,0]\n\t"
        "v_fma_mix_f32 v63, v63, v103, v107 op_sel_hi:[1,0,0]\n\t"
        "v_fma_mix_f32 v64, v64, v100, v104 op_sel_hi:[1,0,0]\n\t"
        "v_fma_mix_f32 v65, v65, v101, v105 op_sel_hi:[1,0,0]\n\t"
        "v_fma_mix_f32 v66, v66, v102, v106 op_sel_hi:[1,0,0]\n\t"
        "v_fma_mix_f32 v67, v67, v103, v107 op_sel_hi:[1,0,0]\n\t"
        "v_cvt_pk_bf16_f32 v92, v60, v64\n\t"
        "v_cvt_pk_bf16_f32 v94, v61, v65\n\t"
        "v_cvt_pk_bf16_f32 v96, v62, v66\n\t"
        "v_cvt_pk_bf16_f32 v98, v63, v67\n\t"
        "buffer_load_dwordx4 v[60:63], v108, %[rq], s52 offen\n\t"
        "s_add_u32 s52, s52, 18432\n\t"
        "buffer_load_dwordx4 v[64:67], v108, %[rq], s52 offen\n\t"
        "v_fma_mix_f32 v68, v68, v100, v104 op_sel_hi:[1,0,0]\n\t"
        "v_fma_mix_f32 v69, v69, v101, v105 op_sel_hi:[1,0,0]\n\t"
        "v_fma_mix_f32 v70, v70, v102, v106 op_sel_hi:[1,0,0]\n\t"
        "v_fma_mix_f32 v71, v71, v103, v107 op_sel_hi:[1,0,0]\n\t"
        "v_fma_mix_f32 v72, v72, v100, v104 op_sel_hi:[1,0,0]\n\t"
        "v_fma_mix_f32 v73, v73, v101, v105 op_sel_hi:[1,0,0]\n\t"
        "v_fma_mix_f32 v74, v74, v102, v106 op_sel_hi:[1,0,0]\n\t"
        "v_fma_mix_f32 v75, v75, v103, v107 op_sel_hi:[1,0,0]\n\t"
        "v_cvt_pk_bf16_f32 v93, v68, v72\n\t"
        "ds_write_b64 v111, v[92:93] offset:0\n\t"
        "v_cvt_pk_bf16_f32 v95, v69, v73\n\t"
        "ds_write_b64 v112, v[94:95] offset:0\n\t"
        "v_cvt_pk_bf16_f32 v97, v70, v74\n\t"
        "ds_write_b64 v113, v[96:97] offset:0\n\t"
        "v_cvt_pk_bf16_f32 v99, v71, v75\n\t"
        "ds_write_b64 v114, v[98:99] offset:0\n\t"
        "s_add_u32 s52, s52, 18432\n\t"
        "buffer_load_dwordx4 v[68:71], v108, %[rq], s52 offen\n\t"
        "s_add_u32 s52, s52, 18432\n\t"
        "buffer_load_dwordx4 v[72:75], v108, %[rq], s52 offen\n\t"
        "s_lshl_b32 s40, s55, 9\n\t"
        "v_add_u32 v124, s40, v121\n\t"
        "ds_read_b128 v[100:103], v124\n\t"
        "ds_read_b128 v[104:107], v124 offset:4608\n\t"
        "s_add_u32 m0, s47, 36864\n\t"
        "s_lshl_b32 s40, s56, 7\n\t"
        "buffer_load_dwordx4 v109, %[rx], s40 offen lds\n\t"
        "s_add_u32 m0, s47, 45056\n\t"
        "s_lshl_b32 s40, s56, 7\n\t"
        "buffer_load_dwordx4 v110, %[rx], s40 offen lds\n\t"
        "s_waitcnt lgkmcnt(2)\n\t"
        "s_waitcnt vmcnt(12)\n\t"
        "L_join0_%=:\n\t"
        "s_barrier\n\t"
        "; interval 1\n\t"
        "ds_read_b128 a[0:3], v119 offset:0\n\t"
        "ds_read_b128 a[4:7], v120 offset:0\n\t"
        "s_waitcnt vmcnt(8)\n\t"
        "v_or3_b32 v123, v122, v76, v77\n\t"
        "ds_read_b128 v[48:51], v115 offset:0\n\t"
        "v_or3_b32 v124, v78, v79, v80\n\t"
        "v_or3_b32 v123, v123, v81, v82\n\t"
        "ds_read_b128 v[52:55], v115 offset:2048\n\t"
        "v_or3_b32 v124, v124, v83, v84\n\t"
        "ds_read_b128 a[8:11], v119 offset:8192\n\t"
        "ds_read_b128 a[12:15], v120 offset:8192\n\t"
        "v_or3_b32 v123, v123, v85, v86\n\t"
        "v_or3_b32 v124, v124, v87, v88\n\t"
        "ds_read_b128 v[56:59], v115 offset:4096\n\t"
        "v_or3_b32 v123, v123, v89, v90\n\t"
        "v_or3_b32 v123, v123, v124, v91\n\t"
        "s_waitcnt lgkmcnt(5)\n\t"
        "s_waitcnt lgkmcnt(4)\n\t"
        "v_mfma_f32_16x16x32_bf16 v[16:19], a[0:3], v[48:51], 0\n\t"
        "v_cmp_lt_u32 vcc, 0x7f, v123\n\t"
        "s_cbranch_vccnz L_slow1_%=\n\t"
        "v_mfma_f32_16x16x32_bf16 v[20:23], a[4:7], v[48:51], 0\n\t"
        "s_mul_i32 s52, s57, 1179648\n\t"
        "v_fma_mix_f32 v76, v76, v100, v104 op_sel_hi:[1,0,0]\n\t"
        "v_fma_mix_f32 v77, v77, v101, v105 op_sel_hi:[1,0,0]\n\t"
        "v_fma_mix_f32 v78, v78, v102, v106 op_sel_hi:[1,0,0]\n\t"
        "ds_read_b128 v[48:51], v115 offset:6144\n\t"
        "v_fma_mix_f32 v79, v79, v103, v107 op_sel_hi:[1,0,0]\n\t"
        "v_fma_mix_f32 v80, v80, v100, v104 op_sel_hi:[1,0,0]\n\t"
        "v_fma_mix_f32 v81, v81, v101, v105 op_sel_hi:[1,0,0]\n\t"
        "s_waitcnt lgkmcnt(4)\n\t"
        "v_mfma_f32_16x16x32_bf16 v[24:27], a[0:3], v[52:55], 0\n\t"
        "v_fma_mix_f32 v82, v82, v102, v106 op_sel_hi:[1,0,0]\n\t"
        "v_fma_mix_f32 v83, v83, v103, v107 op_sel_hi:[1,0,0]\n\t"
        "v_cvt_pk_bf16_f32 v92, v76, v80\n\t"
        "v_mfma_f32_16x16x32_bf16 v[28:31], a[4:7], v[52:55], 0\n\t"
        "v_cvt_pk_bf16_f32 v94, v77, v81\n\t"
        "v_cvt_pk_bf16_f32 v96, v78, v82\n\t"
        "v_cvt_pk_bf16_f32 v98, v79, v83\n\t"
        "buffer_load_dwordx4 v[76:79], v108, %[rq], s52 offen\n\t"
        "ds_read_b128 v[52:55], v116 offset:0\n\t"
        "s_add_u32 s52, s52, 18432\n\t"
        "buffer_load_dwordx4 v[80:83], v108, %[rq], s52 offen\n\t"
        "v_fma_mix_f32 v84, v84, v100, v104 op_sel_hi:[1,0,0]\n\t"
        "v_fma_mix_f32 v85, v85, v101, v105 op_sel_hi:[1,0,0]\n\t"
        "s_waitcnt lgkmcnt(2)\n\t"
        "v_mfma_f32_16x16x32_bf16 v[32:35], a[0:3], v[56:59], 0\n\t"
        "v_fma_mix_f32 v86, v86, v102, v106 op_sel_hi:[1,0,0]\n\t"
        "v_fma_mix_f32 v87, v87, v103, v107 op_sel_hi:[1,0,0]\n\t"
        "v_fma_mix_f32 v88, v88, v100, v104 op_sel_hi:[1,0,0]\n\t"
        "v_mfma_f32_16x16x32_bf16 v[36:39], a[4:7], v[56:59], 0\n\t"
        "v_fma_mix_f32 v89, v89, v101, v105 op_sel_hi:[1,0,0]\n\t"
        "v_fma_mix_f32 v90, v90, v102, v106 op_sel_hi:[1,0,0]\n\t"
        "v_fma_mix_f32 v91, v91, v103, v107 op_sel_hi:[1,0,0]\n\t"
        "ds_read_b128 v[56:59], v116 offset:2048\n\t"
        "v_cvt_pk_bf16_f32 v93, v84, v88\n\t"
        "ds_write_b64 v111, v[92:93] offset:16384\n\t"
        "v_cvt_pk_bf16_f32 v95, v85, v89\n\t"
        "ds_write_b64 v112, v[94:95] offset:16384\n\t"
        "s_waitcnt lgkmcnt(4)\n\t"
        "v_mfma_f32_16x16x32_bf16 v[40:43], a[0:3], v[48:51], 0\n\t"
        "v_cvt_pk_bf16_f32 v97, v86, v90\n\t"
        "ds_write_b64 v113, v[96:97] offset:16384\n\t"
        "v_cvt_pk_bf16_f32 v99, v87, v91\n\t"
        "v_mfma_f32_16x16x32_bf16 v[44:47], a[4:7], v[48:51], 0\n\t"
        "ds_write_b64 v114, v[98:99] offset:16384\n\t"
        "s_add_u32 s52, s52, 18432\n\t"
        "buffer_load_dwordx4 v[84:87], v108, %[rq], s52 offen\n\t"
        "s_add_u32 s52, s52, 18432\n\t"
        "buffer_load_dwordx4 v[88:91], v108, %[rq], s52 offen\n\t"
        "ds_read_b128 v[48:51], v116 offset:4096\n\t"
        "s_lshl_b32 s40, s56, 9\n\t"
        "v_add_u32 v124, s40, v121\n\t"
        "ds_read_b128 v[100:103], v124\n\t"
        "ds_read_b128 v[104:107], v124 offset:4608\n\t"
        "s_add_u32 m0, s47, 55296\n\t"
        "s_lshl_b32 s40, s57, 7\n\t"
        "buffer_load_dwordx4 v109, %[rx], s40 offen lds\n\t"
        "s_add_u32 m0, s47, 63488\n\t"
        "s_lshl_b32 s40, s57, 7\n\t"
        "buffer_load_dwordx4 v110, %[rx], s40 offen lds\n\t"
        "s_waitcnt lgkmcnt(8)\n\t"
        "v_mfma_f32_16x16x32_bf16 v[16:19], a[8:11], v[52:55], v[16:19]\n\t"
        "v_mfma_f32_16x16x32_bf16 v[20:23], a[12:15], v[52:55], v[20:23]\n\t"
        "ds_read_b128 v[52:55], v116 offset:6144\n\t"
        "s_waitcnt lgkmcnt(4)\n\t"
        "s_waitcnt vmcnt(12)\n\t"
        "L_join1_%=:\n\t"
        "s_barrier\n\t"
        "; interval 2\n\t"
        "ds_read_b128 a[16:19], v119 offset:16384\n\t"
        "ds_read_b128 a[20:23], v120 offset:16384\n\t"
        "s_waitcnt vmcnt(8)\n\t"
        "v_or3_b32 v123, v122, v60, v61\n\t"
        "v_mfma_f32_16x16x32_bf16 v[24:27], a[8:11], v[56:59], v[24:27]\n\t"
        "v_or3_b32 v124, v62, v63, v64\n\t"
        "v_or3_b32 v123, v123, v65, v66\n\t"
        "v_mfma_f32_16x16x32_bf16 v[28:31], a[12:15], v[56:59], v[28:31]\n\t"
        "v_or3_b32 v124, v124, v67, v68\n\t"
        "ds_read_b128 v[56:59], v115 offset:18432\n\t"
        "v_or3_b32 v123, v123, v69, v70\n\t"
        "v_or3_b32 v124, v124, v71, v72\n\t"
        "s_waitcnt lgkmcnt(6)\n\t"
        "v_mfma_f32_16x16x32_bf16 v[32:35], a[8:11], v[48:51], v[32:35]\n\t"
        "v_or3_b32 v123, v123, v73, v74\n\t"
        "v_or3_b32 v123, v123, v124, v75\n\t"
        "v_mfma_f32_16x16x32_bf16 v[36:39], a[12:15], v[48:51], v[36:39]\n\t"
        "v_cmp_lt_u32 vcc, 0x7f, v123\n\t"
        "s_waitcnt lgkmcnt(4)\n\t"
        "s_cbranch_vccnz L_slow2_%=\n\t"
        "ds_read_b128 v[48:51], v115 offset:20480\n\t"
        "s_mul_i32 s52, s58, 1179648\n\t"
        "v_fma_mix_f32 v60, v60, v100, v104 op_sel_hi:[1,0,0]\n\t"
        "v_fma_mix_f32 v61, v61, v101, v105 op_sel_hi:[1,0,0]\n\t"
        "ds_read_b128 a[24:27], v119 offset:24576\n\t"
        "ds_read_b128 a[28:31], v120 offset:24576\n\t"
        "v_fma_mix_f32 v62, v62, v102, v106 op_sel_hi:[1,0,0]\n\t"
        "v_fma_mix_f32 v63, v63, v103, v107 op_sel_hi:[1,0,0]\n\t"
        "s_waitcnt lgkmcnt(6)\n\t"
        "v_mfma_f32_16x16x32_bf16 v[40:43], a[8:11], v[52:55], v[40:43]\n\t"
        "v_fma_mix_f32 v64, v64, v100, v104 op_sel_hi:[1,0,0]\n\t"
        "v_fma_mix_f32 v65, v65, v101, v105 op_sel_hi:[1,0,0]\n\t"
        "v_mfma_f32_16x16x32_bf16 v[44:47], a[12:15], v[52:55], v[44:47]\n\t"
        "v_fma_mix_f32 v66, v66, v102, v106 op_sel_hi:[1,0,0]\n\t"
        "v_fma_mix_f32 v67, v67, v103, v107 op_sel_hi:[1,0,0]\n\t"
        "ds_read_b128 v[52:55], v115 offset:22528\n\t"
        "v_cvt_pk_bf16_f32 v92, v60, v64\n\t"
        "v_cvt_pk_bf16_f32 v94, v61, v65\n\t"
        "s_waitcnt lgkmcnt(5)\n\t"
        "s_waitcnt lgkmcnt(4)\n\t"
        "v_mfma_f32_16x16x32_bf16 v[16:19], a[16:19], v[56:59], v[16:19]\n\t"
        "v_cvt_pk_bf16_f32 v96, v62, v66\n\t"
        "v_cvt_pk_bf16_f32 v98, v63, v67\n\t"
        "v_mfma_f32_16x16x32_bf16 v[20:23], a[20:23], v[56:59], v[20:23]\n\t"
        "buffer_load_dwordx4 v[60:63], v108, %[rq], s52 offen\n\t"
        "s_add_u32 s52, s52, 18432\n\t"
        "buffer_load_dwordx4 v[64:67], v108, %[rq], s52 offen\n\t"
        "ds_read_b128 v[56:59], v115 offset:24576\n\t"
        "v_fma_mix_f32 v68, v68, v100, v104 op_sel_hi:[1,0,0]\n\t"
        "v_fma_mix_f32 v69, v69, v101, v105 op_sel_hi:[1,0,0]\n\t"
        "s_waitcnt lgkmcnt(4)\n\t"
        "v_mfma_f32_16x16x32_bf16 v[24:27], a[16:19], v[48:51], v[24:27]\n\t"
        "v_fma_mix_f32 v70, v70, v102, v106 op_sel_hi:[1,0,0]\n\t"
        "v_fma_mix_f32 v71, v71, v103, v107 op_sel_hi:[1,0,0]\n\t"
        "v_fma_mix_f32 v72, v72, v100, v104 op_sel_hi:[1,0,0]\n\t"
        "v_mfma_f32_16x16x32_bf16 v[28:31], a[20:23], v[48:51], v[28:31]\n\t"
        "v_fma_mix_f32 v73, v73, v101, v105 op_sel_hi:[1,0,0]\n\t"
        "v_fma_mix_f32 v74, v74, v102, v106 op_sel_hi:[1,0,0]\n\t"
        "ds_read_b128 v[48:51], v116 offset:18432\n\t"
        "v_fma_mix_f32 v75, v75, v103, v107 op_sel_hi:[1,0,0]\n\t"
        "v_cvt_pk_bf16_f32 v93, v68, v72\n\t"
        "s_waitcnt lgkmcnt(2)\n\t"
        "v_mfma_f32_16x16x32_bf16 v[32:35], a[16:19], v[52:55], v[32:35]\n\t"
        "ds_write_b64 v111, v[92:93] offset:0\n\t"
        "v_cvt_pk_bf16_f32 v95, v69, v73\n\t"
        "v_mfma_f32_16x16x32_bf16 v[36:39], a[20:23], v[52:55], v[36:39]\n\t"
        "ds_write_b64 v112, v[94:95] offset:0\n\t"
        "v_cvt_pk_bf16_f32 v97, v70, v74\n\t"
        "ds_read_b128 v[52:55], v116 offset:20480\n\t"
        "ds_write_b64 v113, v[96:97] offset:0\n\t"
        "v_cvt_pk_bf16_f32 v99, v71, v75\n\t"
        "s_waitcnt lgkmcnt(5)\n\t"
        "v_mfma_f32_16x16x32_bf16 v[40:43], a[16:19], v[56:59], v[40:43]\n\t"
        "ds_write_b64 v114, v[98:99] offset:0\n\t"
        "s_add_u32 s52, s52, 18432\n\t"
        "buffer_load_dwordx4 v[68:71], v108, %[rq], s52 offen\n\t"
        "v_mfma_f32_16x16x32_bf16 v[44:47], a[20:23], v[56:59], v[44:47]\n\t"
        "s_add_u32 s52, s52, 18432\n\t"
        "buffer_load_dwordx4 v[72:75], v108, %[rq], s52 offen\n\t"
        "s_lshl_b32 s40, s57, 9\n\t"
        "v_add_u32 v124, s40, v121\n\t"
        "ds_read_b128 v[100:103], v124\n\t"
        "ds_read_b128 v[104:107], v124 offset:4608\n\t"
        "ds_read_b128 v[56:59], v116 offset:22528\n\t"
        "s_add_u32 m0, s47, 73728\n\t"
        "s_lshl_b32 s40, s58, 7\n\t"
        "buffer_load_dwordx4 v109, %[rx], s40 offen lds\n\t"
        "s_add_u32 m0, s47, 81920\n\t"
        "s_lshl_b32 s40, s58, 7\n\t"
        "buffer_load_dwordx4 v110, %[rx], s40 offen lds\n\t"
        "s_waitcnt lgkmcnt(8)\n\t"
        "v_mfma_f32_16x16x32_bf16 v[16:19], a[24:27], v[48:51], v[16:19]\n\t"
        "v_mfma_f32_16x16x32_bf16 v[20:23], a[28:31], v[48:51], v[20:23]\n\t"
        "ds_read_b128 v[48:51], v116 offset:24576\n\t"
        "s_waitcnt lgkmcnt(4)\n\t"
        "s_waitcnt vmcnt(12)\n\t"
        "L_join2_%=:\n\t"
        "s_barrier\n\t"
        "; interval 3\n\t"
        "ds_read_b128 a[32:35], v119 offset:0\n\t"
        "ds_read_b128 a[36:39], v120 offset:0\n\t"
        "s_waitcnt vmcnt(8)\n\t"
        "v_or3_b32 v123, v122, v76, v77\n\t"
        "v_mfma_f32_16x16x32_bf16 v[24:27], a[24:27], v[52:55], v[24:27]\n\t"
        "v_or3_b32 v124, v78, v79, v80\n\t"
        "v_or3_b32 v123, v123, v81, v82\n\t"
        "v_mfma_f32_16x16x32_bf16 v[28:31], a[28:31], v[52:55], v[28:31]\n\t"
        "v_or3_b32 v124, v124, v83, v84\n\t"
        "ds_read_b128 v[52:55], v115 offset:36864\n\t"
        "v_or3_b32 v123, v123, v85, v86\n\t"
        "v_or3_b32 v124, v124, v87, v88\n\t"
        "s_waitcnt lgkmcnt(4)\n\t"
        "v_mfma_f32_16x16x32_bf16 v[32:35], a[24:27], v[56:59], v[32:35]\n\t"
        "v_or3_b32 v123, v123, v89, v90\n\t"
        "v_or3_b32 v123, v123, v124, v91\n\t"
        "v_mfma_f32_16x16x32_bf16 v[36:39], a[28:31], v[56:59], v[36:39]\n\t"
        "v_cmp_lt_u32 vcc, 0x7f, v123\n\t"
        "s_cbranch_vccnz L_slow3_%=\n\t"
        "ds_read_b128 v[56:59], v115 offset:38912\n\t"
        "s_mul_i32 s52, s59, 1179648\n\t"
        "v_fma_mix_f32 v76, v76, v100, v104 op_sel_hi:[1,0,0]\n\t"
        "v_fma_mix_f32 v77, v77, v101, v105 op_sel_hi:[1,0,0]\n\t"
        "ds_read_b128 a[40:43], v119 offset:8192\n\t"
        "ds_read_b128 a[44:47], v120 offset:8192\n\t"
        "v_fma_mix_f32 v78, v78, v102, v106 op_sel_hi:[1,0,0]\n\t"
        "v_fma_mix_f32 v79, v79, v103, v107 op_sel_hi:[1,0,0]\n\t"
        "s_waitcnt lgkmcnt(6)\n\t"
        "v_mfma_f32_16x16x32_bf16 v[40:43], a[24:27], v[48:51], v[40:43]\n\t"
        "v_fma_mix_f32 v80, v80, v100, v104 op_sel_hi:[1,0,0]\n\t"
        "v_fma_mix_f32 v81, v81, v101, v105 op_sel_hi:[1,0,0]\n\t"
        "v_mfma_f32_16x16x32_bf16 v[44:47], a[28:31], v[48:51], v[44:47]\n\t"
        "v_fma_mix_f32 v82, v82, v102, v106 op_sel_hi:[1,0,0]\n\t"
        "v_fma_mix_f32 v83, v83, v103, v107 op_sel_hi:[1,0,0]\n\t"
        "ds_read_b128 v[48:51], v115 offset:40960\n\t"
        "v_cvt_pk_bf16_f32 v92, v76, v80\n\t"
        "v_cvt_pk_bf16_f32 v94, v77, v81\n\t"
        "s_waitcnt lgkmcnt(5)\n\t"
        "s_waitcnt lgkmcnt(4)\n\t"
        "v_mfma_f32_16x16x32_bf16 v[16:19], a[32:35], v[52:55], v[16:19]\n\t"
        "v_cvt_pk_bf16_f32 v96, v78, v82\n\t"
        "v_cvt_pk_bf16_f32 v98, v79, v83\n\t"
        "v_mfma_f32_16x16x32_bf16 v[20:23], a[36:39], v[52:55], v[20:23]\n\t"
        "buffer_load_dwordx4 v[76:79], v108, %[rq], s52 offen\n\t"
        "s_add_u32 s52, s52, 18432\n\t"
        "buffer_load_dwordx4 v[80:83], v108, %[rq], s52 offen\n\t"
        "ds_read_b128 v[52:55], v115 offset:43008\n\t"
        "v_fma_mix_f32 v84, v84, v100, v104 op_sel_hi:[1,0,0]\n\t"
        "v_fma_mix_f32 v85, v85, v101, v105 op_sel_hi:[1,0,0]\n\t"
        "s_waitcnt lgkmcnt(4)\n\t"
        "v_mfma_f32_16x16x32_bf16 v[24:27], a[32:35], v[56:59], v[24:27]\n\t"
        "v_fma_mix_f32 v86, v86, v102, v106 op_sel_hi:[1,0,0]\n\t"
        "v_fma_mix_f32 v87, v87, v103, v107 op_sel_hi:[1,0,0]\n\t"
        "v_fma_mix_f32 v88, v88, v100, v104 op_sel_hi:[1,0,0]\n\t"
        "v_mfma_f32_16x16x32_bf16 v[28:31], a[36:39], v[56:59], v[28:31]\n\t"
        "v_fma_mix_f32 v89, v89, v101, v105 op_sel_hi:[1,0,0]\n\t"
        "v_fma_mix_f32 v90, v90, v102, v106 op_sel_hi:[1,0,0]\n\t"
        "ds_read_b128 v[56:59], v116 offset:36864\n\t"
        "v_fma_mix_f32 v91, v91, v103, v107 op_sel_hi:[1,0,0]\n\t"
        "v_cvt_pk_bf16_f32 v93, v84, v88\n\t"
        "s_waitcnt lgkmcnt(2)\n\t"
        "v_mfma_f32_16x16x32_bf16 v[32:35], a[32:35], v[48:51], v[32:35]\n\t"
        "ds_write_b64 v111, v[92:93] offset:16384\n\t"
        "v_cvt_pk_bf16_f32 v95, v85, v89\n\t"
        "v_mfma_f32_16x16x32_bf16 v[36:39], a[36:39], v[48:51], v[36:39]\n\t"
        "ds_write_b64 v112, v[94:95] offset:16384\n\t"
        "v_cvt_pk_bf16_f32 v97, v86, v90\n\t"
        "ds_read_b128 v[48:51], v116 offset:38912\n\t"
        "ds_write_b64 v113, v[96:97] offset:16384\n\t"
        "v_cvt_pk_bf16_f32 v99, v87, v91\n\t"
        "s_waitcnt lgkmcnt(5)\n\t"
        "v_mfma_f32_16x16x32_bf16 v[40:43], a[32:35], v[52:55], v[40:43]\n\t"
        "ds_write_b64 v114, v[98:99] offset:16384\n\t"
        "s_add_u32 s52, s52, 18432\n\t"
        "buffer_load_dwordx4 v[84:87], v108, %[rq], s52 offen\n\t"
        "v_mfma_f32_16x16x32_bf16 v[44:47], a[36:39], v[52:55], v[44:47]\n\t"
        "s_add_u32 s52, s52, 18432\n\t"
        "buffer_load_dwordx4 v[88:91], v108, %[rq], s52 offen\n\t"
        "s_lshl_b32 s40, s58, 9\n\t"
        "v_add_u32 v124, s40, v121\n\t"
        "ds_read_b128 v[100:103], v124\n\t"
        "ds_read_b128 v[104:107], v124 offset:4608\n\t"
        "ds_read_b128 v[52:55], v116 offset:40960\n\t"
        "s_add_u32 m0, s47, 92160\n\t"
        "s_lshl_b32 s40, s59, 7\n\t"
        "buffer_load_dwordx4 v109, %[rx], s40 offen lds\n\t"
        "s_add_u32 m0, s47, 100352\n\t"
        "s_lshl_b32 s40, s59, 7\n\t"
        "buffer_load_dwordx4 v110, %[rx], s40 offen lds\n\t"
        "s_waitcnt lgkmcnt(8)\n\t"
        "v_mfma_f32_16x16x32_bf16 v[16:19], a[40:43], v[56:59], v[16:19]\n\t"
        "v_mfma_f32_16x16x32_bf16 v[20:23], a[44:47], v[56:59], v[20:23]\n\t"
        "ds_read_b128 v[56:59], v116 offset:43008\n\t"
        "s_waitcnt lgkmcnt(4)\n\t"
        "s_waitcnt vmcnt(12)\n\t"
        "L_join3_%=:\n\t"
        "s_barrier\n\t"
        "; interval 4\n\t"
        "ds_read_b128 a[48:51], v119 offset:16384\n\t"
        "ds_read_b128 a[52:55], v120 offset:16384\n\t"
        "s_waitcnt vmcnt(8)\n\t"
        "v_or3_b32 v123, v122, v60, v61\n\t"
        "v_mfma_f32_16x16x32_bf16 v[24:27], a[40:43], v[48:51], v[24:27]\n\t"
        "v_or3_b32 v124, v62, v63, v64\n\t"
        "v_or3_b32 v123, v123, v65, v66\n\t"
        "v_mfma_f32_16x16x32_bf16 v[28:31], a[44:47], v[48:51], v[28:31]\n\t"
        "v_or3_b32 v124, v124, v67, v68\n\t"
        "ds_read_b128 v[48:51], v117 offset:0\n\t"
        "v_or3_b32 v123, v123, v69, v70\n\t"
        "v_or3_b32 v124, v124, v71, v72\n\t"
        "s_waitcnt lgkmcnt(4)\n\t"
        "v_mfma_f32_16x16x32_bf16 v[32:35], a[40:43], v[52:55], v[32:35]\n\t"
        "v_or3_b32 v123, v123, v73, v74\n\t"
        "v_or3_b32 v123, v123, v124, v75\n\t"
        "v_mfma_f32_16x16x32_bf16 v[36:39], a[44:47], v[52:55], v[36:39]\n\t"
        "v_cmp_lt_u32 vcc, 0x7f, v123\n\t"
        "s_cbranch_vccnz L_slow4_%=\n\t"
        "ds_read_b128 v[52:55], v117 offset:2048\n\t"
        "s_mul_i32 s52, s60, 1179648\n\t"
        "v_fma_mix_f32 v60, v60, v100, v104 op_sel_hi:[1,0,0]\n\t"
        "v_fma_mix_f32 v61, v61, v101, v105 op_sel_hi:[1,0,0]\n\t"
        "ds_read_b128 a[56:59], v119 offset:24576\n\t"
        "ds_read_b128 a[60:63], v120 offset:24576\n\t"
        "v_fma_mix_f32 v62, v62, v102, v106 op_sel_hi:[1,0,0]\n\t"
        "v_fma_mix_f32 v63, v63, v103, v107 op_sel_hi:[1,0,0]\n\t"
        "s_waitcnt lgkmcnt(6)\n\t"
        "v_mfma_f32_16x16x32_bf16 v[40:43], a[40:43], v[56:59], v[40:43]\n\t"
        "v_fma_mix_f32 v64, v64, v100, v104 op_sel_hi:[1,0,0]\n\t"
        "v_fma_mix_f32 v65, v65, v101, v105 op_sel_hi:[1,0,0]\n\t"
        "v_mfma_f32_16x16x32_bf16 v[44:47], a[44:47], v[56:59], v[44:47]\n\t"
        "v_fma_mix_f32 v66, v66, v102, v106 op_sel_hi:[1,0,0]\n\t"
        "v_fma_mix_f32 v67, v67, v103, v107 op_sel_hi:[1,0,0]\n\t"
        "ds_read_b128 v[56:59], v117 offset:4096\n\t"
        "v_cvt_pk_bf16_f32 v92, v60, v64\n\t"
        "v_cvt_pk_bf16_f32 v94, v61, v65\n\t"
        "s_waitcnt lgkmcnt(5)\n\t"
        "s_waitcnt lgkmcnt(4)\n\t"
        "v_mfma_f32_16x16x32_bf16 v[16:19], a[48:51], v[48:51], v[16:19]\n\t"
        "v_cvt_pk_bf16_f32 v96, v62, v66\n\t"
        "v_cvt_pk_bf16_f32 v98, v63, v67\n\t"
        "v_mfma_f32_16x16x32_bf16 v[20:23], a[52:55], v[48:51], v[20:23]\n\t"
        "buffer_load_dwordx4 v[60:63], v108, %[rq], s52 offen\n\t"
        "s_add_u32 s52, s52, 18432\n\t"
        "buffer_load_dwordx4 v[64:67], v108, %[rq], s52 offen\n\t"
        "ds_read_b128 v[48:51], v117 offset:6144\n\t"
        "v_fma_mix_f32 v68, v68, v100, v104 op_sel_hi:[1,0,0]\n\t"
        "v_fma_mix_f32 v69, v69, v101, v105 op_sel_hi:[1,0,0]\n\t"
        "s_waitcnt lgkmcnt(4)\n\t"
        "v_mfma_f32_16x16x32_bf16 v[24:27], a[48:51], v[52:55], v[24:27]\n\t"
        "v_fma_mix_f32 v70, v70, v102, v106 op_sel_hi:[1,0,0]\n\t"
        "v_fma_mix_f32 v71, v71, v103, v107 op_sel_hi:[1,0,0]\n\t"
        "v_fma_mix_f32 v72, v72, v100, v104 op_sel_hi:[1,0,0]\n\t"
        "v_mfma_f32_16x16x32_bf16 v[28:31], a[52:55], v[52:55], v[28:31]\n\t"
        "v_fma_mix_f32 v73, v73, v101, v105 op_sel_hi:[1,0,0]\n\t"
        "v_fma_mix_f32 v74, v74, v102, v106 op_sel_hi:[1,0,0]\n\t"
        "ds_read_b128 v[52:55], v118 offset:0\n\t"
        "v_fma_mix_f32 v75, v75, v103, v107 op_sel_hi:[1,0,0]\n\t"
        "v_cvt_pk_bf16_f32 v93, v68, v72\n\t"
        "s_waitcnt lgkmcnt(2)\n\t"
        "v_mfma_f32_16x16x32_bf16 v[32:35], a[48:51], v[56:59], v[32:35]\n\t"
        "ds_write_b64 v111, v[92:93] offset:0\n\t"
        "v_cvt_pk_bf16_f32 v95, v69, v73\n\t"
        "v_mfma_f32_16x16x32_bf16 v[36:39], a[52:55], v[56:59], v[36:39]\n\t"
        "ds_write_b64 v112, v[94:95] offset:0\n\t"
        "v_cvt_pk_bf16_f32 v97, v70, v74\n\t"
        "ds_read_b128 v[56:59], v118 offset:2048\n\t"
        "ds_write_b64 v113, v[96:97] offset:0\n\t"
        "v_cvt_pk_bf16_f32 v99, v71, v75\n\t"
        "s_waitcnt lgkmcnt(5)\n\t"
        "v_mfma_f32_16x16x32_bf16 v[40:43], a[48:51], v[48:51], v[40:43]\n\t"
        "ds_write_b64 v114, v[98:99] offset:0\n\t"
        "s_add_u32 s52, s52, 18432\n\t"
        "buffer_load_dwordx4 v[68:71], v108, %[rq], s52 offen\n\t"
        "v_mfma_f32_16x16x32_bf16 v[44:47], a[52:55], v[48:51], v[44:47]\n\t"
        "s_add_u32 s52, s52, 18432\n\t"
        "buffer_load_dwordx4 v[72:75], v108, %[rq], s52 offen\n\t"
        "s_lshl_b32 s40, s59, 9\n\t"
        "v_add_u32 v124, s40, v121\n\t"
        "ds_read_b128 v[100:103], v124\n\t"
        "ds_read_b128 v[104:107], v124 offset:4608\n\t"
        "ds_read_b128 v[48:51], v118 offset:4096\n\t"
        "s_add_u32 m0, s47, 0\n\t"
        "s_lshl_b32 s40, s60, 7\n\t"
        "buffer_load_dwordx4 v109, %[rx], s40 offen lds\n\t"
        "s_add_u32 m0, s47, 8192\n\t"
        "s_lshl_b32 s40, s60, 7\n\t"
        "buffer_load_dwordx4 v110, %[rx], s40 offen lds\n\t"
        "s_waitcnt lgkmcnt(8)\n\t"
        "v_mfma_f32_16x16x32_bf16 v[16:19], a[56:59], v[52:55], v[16:19]\n\t"
        "v_mfma_f32_16x16x32_bf16 v[20:23], a[60:63], v[52:55], v[20:23]\n\t"
        "ds_read_b128 v[52:55], v118 offset:6144\n\t"
        "s_waitcnt lgkmcnt(4)\n\t"
        "s_waitcnt vmcnt(12)\n\t"
        "L_join4_%=:\n\t"
        "s_barrier\n\t"
        "; interval 5\n\t"
        "ds_read_b128 a[64:67], v119 offset:0\n\t"
        "ds_read_b128 a[68:71], v120 offset:0\n\t"
        "s_waitcnt vmcnt(8)\n\t"
        "v_or3_b32 v123, v122, v76, v77\n\t"
        "v_mfma_f32_16x16x32_bf16 v[24:27], a[56:59], v[56:59], v[24:27]\n\t"
        "v_or3_b32 v124, v78, v79, v80\n\t"
        "v_or3_b32 v123, v123, v81, v82\n\t"
        "v_mfma_f32_16x16x32_bf16 v[28:31], a[60:63], v[56:59], v[28:31]\n\t"
        "v_or3_b32 v124, v124, v83, v84\n\t"
        "ds_read_b128 v[56:59], v117 offset:18432\n\t"
        "v_or3_b32 v123, v123, v85, v86\n\t"
        "v_or3_b32 v124, v124, v87, v88\n\t"
        "s_waitcnt lgkmcnt(4)\n\t"
        "v_mfma_f32_16x16x32_bf16 v[32:35], a[56:59], v[48:51], v[32:35]\n\t"
        "v_or3_b32 v123, v123, v89, v90\n\t"
        "v_or3_b32 v123, v123, v124, v91\n\t"
        "v_mfma_f32_16x16x32_bf16 v[36:39], a[60:63], v[48:51], v[36:39]\n\t"
        "v_cmp_lt_u32 vcc, 0x7f, v123\n\t"
        "s_cbranch_vccnz L_slow5_%=\n\t"
        "ds_read_b128 v[48:51], v117 offset:20480\n\t"
        "s_mul_i32 s52, s61, 1179648\n\t"
        "v_fma_mix_f32 v76, v76, v100, v104 op_sel_hi:[1,0,0]\n\t"
        "v_fma_mix_f32 v77, v77, v101, v105 op_sel_hi:[1,0,0]\n\t"
        "ds_read_b128 a[72:75], v119 offset:8192\n\t"
        "ds_read_b128 a[76:79], v120 offset:8192\n\t"
        "v_fma_mix_f32 v78, v78, v102, v106 op_sel_hi:[1,0,0]\n\t"
        "v_fma_mix_f32 v79, v79, v103, v107 op_sel_hi:[1,0,0]\n\t"
        "s_waitcnt lgkmcnt(6)\n\t"
        "v_mfma_f32_16x16x32_bf16 v[40:43], a[56:59], v[52:55], v[40:43]\n\t"
        "v_fma_mix_f32 v80, v80, v100, v104 op_sel_hi:[1,0,0]\n\t"
        "v_fma_mix_f32 v81, v81, v101, v105 op_sel_hi:[1,0,0]\n\t"
        "v_mfma_f32_16x16x32_bf16 v[44:47], a[60:63], v[52:55], v[44:47]\n\t"
        "v_fma_mix_f32 v82, v82, v102, v106 op_sel_hi:[1,0,0]\n\t"
        "v_fma_mix_f32 v83, v83, v103, v107 op_sel_hi:[1,0,0]\n\t"
        "ds_read_b128 v[52:55], v117 offset:22528\n\t"
        "v_cvt_pk_bf16_f32 v92, v76, v80\n\t"
        "v_cvt_pk_bf16_f32 v94, v77, v81\n\t"
        "s_waitcnt lgkmcnt(5)\n\t"
        "s_waitcnt lgkmcnt(4)\n\t"
        "v_mfma_f32_16x16x32_bf16 v[16:19], a[64:67], v[56:59], v[16:19]\n\t"
        "v_cvt_pk_bf16_f32 v96, v78, v82\n\t"
        "v_cvt_pk_bf16_f32 v98, v79, v83\n\t"
        "v_mfma_f32_16x16x32_bf16 v[20:23], a[68:71], v[56:59], v[20:23]\n\t"
        "buffer_load_dwordx4 v[76:79], v108, %[rq], s52 offen\n\t"
        "s_add_u32 s52, s52, 18432\n\t"
        "buffer_load_dwordx4 v[80:83], v108, %[rq], s52 offen\n\t"
        "ds_read_b128 v[56:59], v117 offset:24576\n\t"
        "v_fma_mix_f32 v84, v84, v100, v104 op_sel_hi:[1,0,0]\n\t"
        "v_fma_mix_f32 v85, v85, v101, v105 op_sel_hi:[1,0,0]\n\t"
        "s_waitcnt lgkmcnt(4)\n\t"
        "v_mfma_f32_16x16x32_bf16 v[24:27], a[64:67], v[48:51], v[24:27]\n\t"
        "v_fma_mix_f32 v86, v86, v102, v106 op_sel_hi:[1,0,0]\n\t"
        "v_fma_mix_f32 v87, v87, v103, v107 op_sel_hi:[1,0,0]\n\t"
        "v_fma_mix_f32 v88, v88, v100, v104 op_sel_hi:[1,0,0]\n\t"
        "v_mfma_f32_16x16x32_bf16 v[28:31], a[68:71], v[48:51], v[28:31]\n\t"
        "v_fma_mix_f32 v89, v89, v101, v105 op_sel_hi:[1,0,0]\n\t"
        "v_fma_mix_f32 v90, v90, v102, v106 op_sel_hi:[1,0,0]\n\t"
        "ds_read_b128 v[48:51], v118 offset:18432\n\t"
        "v_fma_mix_f32 v91, v91, v103, v107 op_sel_hi:[1,0,0]\n\t"
        "v_cvt_pk_bf16_f32 v93, v84, v88\n\t"
        "s_waitcnt lgkmcnt(2)\n\t"
        "v_mfma_f32_16x16x32_bf16 v[32:35], a[64:67], v[52:55], v[32:35]\n\t"
        "ds_write_b64 v111, v[92:93] offset:16384\n\t"
        "v_cvt_pk_bf16_f32 v95, v85, v89\n\t"
        "v_mfma_f32_16x16x32_bf16 v[36:39], a[68:71], v[52:55], v[36:39]\n\t"
        "ds_write_b64 v112, v[94:95] offset:16384\n\t"
        "v_cvt_pk_bf16_f32 v97, v86, v90\n\t"
        "ds_read_b128 v[52:55], v118 offset:20480\n\t"
        "ds_write_b64 v113, v[96:97] offset:16384\n\t"
        "v_cvt_pk_bf16_f32 v99, v87, v91\n\t"
        "s_waitcnt lgkmcnt(5)\n\t"
        "v_mfma_f32_16x16x32_bf16 v[40:43], a[64:67], v[56:59], v[40:43]\n\t"
        "ds_write_b64 v114, v[98:99] offset:16384\n\t"
        "s_add_u32 s52, s52, 18432\n\t"
        "buffer_load_dwordx4 v[84:87], v108, %[rq], s52 offen\n\t"
        "v_mfma_f32_16x16x32_bf16 v[44:47], a[68:71], v[56:59], v[44:47]\n\t"
        "s_add_u32 s52, s52, 18432\n\t"
        "buffer_load_dwordx4 v[88:91], v108, %[rq], s52 offen\n\t"
        "s_lshl_b32 s40, s60, 9\n\t"
        "v_add_u32 v124, s40, v121\n\t"
        "ds_read_b128 v[100:103], v124\n\t"
        "ds_read_b128 v[104:107], v124 offset:4608\n\t"
        "ds_read_b128 v[56:59], v118 offset:22528\n\t"
        "s_add_u32 m0, s47, 18432\n\t"
        "s_lshl_b32 s40, s61, 7\n\t"
        "buffer_load_dwordx4 v109, %[rx], s40 offen lds\n\t"
        "s_add_u32 m0, s47, 26624\n\t"
        "s_lshl_b32 s40, s61, 7\n\t"
        "buffer_load_dwordx4 v110, %[rx], s40 offen lds\n\t"
        "s_waitcnt lgkmcnt(8)\n\t"
        "v_mfma_f32_16x16x32_bf16 v[16:19], a[72:75], v[48:51], v[16:19]\n\t"
        "v_mfma_f32_16x16x32_bf16 v[20:23], a[76:79], v[48:51], v[20:23]\n\t"
        "ds_read_b128 v[48:51], v118 offset:24576\n\t"
        "s_waitcnt lgkmcnt(4)\n\t"
        "s_waitcnt vmcnt(12)\n\t"
        "L_join5_%=:\n\t"
        "s_barrier\n\t"
        "; interval 6\n\t"
        "ds_read_b128 a[80:83], v119 offset:16384\n\t"
        "ds_read_b128 a[84:87], v120 offset:16384\n\t"
        "s_waitcnt vmcnt(8)\n\t"
        "v_or3_b32 v123, v122, v60, v61\n\t"
        "v_mfma_f32_16x16x32_bf16 v[24:27], a[72:75], v[52:55], v[24:27]\n\t"
        "v_or3_b32 v124, v62, v63, v64\n\t"
        "v_or3_b32 v123, v123, v65, v66\n\t"
        "v_mfma_f32_16x16x32_bf16 v[28:31], a[76:79], v[52:55], v[28:31]\n\t"
        "v_or3_b32 v124, v124, v67, v68\n\t"
        "ds_read_b128 v[52:55], v117 offset:36864\n\t"
        "v_or3_b32 v123, v123, v69, v70\n\t"
        "v_or3_b32 v124, v124, v71, v72\n\t"
        "s_waitcnt lgkmcnt(4)\n\t"
        "v_mfma_f32_16x16x32_bf16 v[32:35], a[72:75], v[56:59], v[32:35]\n\t"
        "v_or3_b32 v123, v123, v73, v74\n\t"
        "v_or3_b32 v123, v123, v124, v75\n\t"
        "v_mfma_f32_16x16x32_bf16 v[36:39], a[76:79], v[56:59], v[36:39]\n\t"
        "v_cmp_lt_u32 vcc, 0x7f, v123\n\t"
        "s_cbranch_vccnz L_slow6_%=\n\t"
        "ds_read_b128 v[56:59], v117 offset:38912\n\t"
        "s_mul_i32 s52, s62, 1179648\n\t"
        "v_fma_mix_f32 v60, v60, v100, v104 op_sel_hi:[1,0,0]\n\t"
        "v_fma_mix_f32 v61, v61, v101, v105 op_sel_hi:[1,0,0]\n\t"
        "ds_read_b128 a[88:91], v119 offset:24576\n\t"
        "ds_read_b128 a[92:95], v120 offset:24576\n\t"
        "v_fma_mix_f32 v62, v62, v102, v106 op_sel_hi:[1,0,0]\n\t"
        "v_fma_mix_f32 v63, v63, v103, v107 op_sel_hi:[1,0,0]\n\t"
        "s_waitcnt lgkmcnt(6)\n\t"
        "v_mfma_f32_16x16x32_bf16 v[40:43], a[72:75], v[48:51], v[40:43]\n\t"
        "v_fma_mix_f32 v64, v64, v100, v104 op_sel_hi:[1,0,0]\n\t"
        "v_fma_mix_f32 v65, v65, v101, v105 op_sel_hi:[1,0,0]\n\t"
        "v_mfma_f32_16x16x32_bf16 v[44:47], a[76:79], v[48:51], v[44:47]\n\t"
        "v_fma_mix_f32 v66, v66, v102, v106 op_sel_hi:[1,0,0]\n\t"
        "v_fma_mix_f32 v67, v67, v103, v107 op_sel_hi:[1,0,0]\n\t"
        "ds_read_b128 v[48:51], v117 offset:40960\n\t"
        "v_cvt_pk_bf16_f32 v92, v60, v64\n\t"
        "v_cvt_pk_bf16_f32 v94, v61, v65\n\t"
        "s_waitcnt lgkmcnt(5)\n\t"
        "s_waitcnt lgkmcnt(4)\n\t"
        "v_mfma_f32_16x16x32_bf16 v[16:19], a[80:83], v[52:55], v[16:19]\n\t"
        "v_cvt_pk_bf16_f32 v96, v62, v66\n\t"
        "v_cvt_pk_bf16_f32 v98, v63, v67\n\t"
        "v_mfma_f32_16x16x32_bf16 v[20:23], a[84:87], v[52:55], v[20:23]\n\t"
        "buffer_load_dwordx4 v[60:63], v108, %[rq], s52 offen\n\t"
        "s_add_u32 s52, s52, 18432\n\t"
        "buffer_load_dwordx4 v[64:67], v108, %[rq], s52 offen\n\t"
        "ds_read_b128 v[52:55], v117 offset:43008\n\t"
        "v_fma_mix_f32 v68, v68, v100, v104 op_sel_hi:[1,0,0]\n\t"
        "v_fma_mix_f32 v69, v69, v101, v105 op_sel_hi:[1,0,0]\n\t"
        "s_waitcnt lgkmcnt(4)\n\t"
        "v_mfma_f32_16x16x32_bf16 v[24:27], a[80:83], v[56:59], v[24:27]\n\t"
        "v_fma_mix_f32 v70, v70, v102, v106 op_sel_hi:[1,0,0]\n\t"
        "v_fma_mix_f32 v71, v71, v103, v107 op_sel_hi:[1,0,0]\n\t"
        "v_fma_mix_f32 v72, v72, v100, v104 op_sel_hi:[1,0,0]\n\t"
        "v_mfma_f32_16x16x32_bf16 v[28:31], a[84:87], v[56:59], v[28:31]\n\t"
        "v_fma_mix_f32 v73, v73, v101, v105 op_sel_hi:[1,0,0]\n\t"
        "v_fma_mix_f32 v74, v74, v102, v106 op_sel_hi:[1,0,0]\n\t"
        "ds_read_b128 v[56:59], v118 offset:36864\n\t"
        "v_fma_mix_f32 v75, v75, v103, v107 op_sel_hi:[1,0,0]\n\t"
        "v_cvt_pk_bf16_f32 v93, v68, v72\n\t"
        "s_waitcnt lgkmcnt(2)\n\t"
        "v_mfma_f32_16x16x32_bf16 v[32:35], a[80:83], v[48:51], v[32:35]\n\t"
        "ds_write_b64 v111, v[92:93] offset:0\n\t"
        "v_cvt_pk_bf16_f32 v95, v69, v73\n\t"
        "v_mfma_f32_16x16x32_bf16 v[36:39], a[84:87], v[48:51], v[36:39]\n\t"
        "ds_write_b64 v112, v[94:95] offset:0\n\t"
        "v_cvt_pk_bf16_f32 v97, v70, v74\n\t"
        "ds_read_b128 v[48:51], v118 offset:38912\n\t"
        "ds_write_b64 v113, v[96:97] offset:0\n\t"
        "v_cvt_pk_bf16_f32 v99, v71, v75\n\t"
        "s_waitcnt lgkmcnt(5)\n\t"
        "v_mfma_f32_16x16x32_bf16 v[40:43], a[80:83], v[52:55], v[40:43]\n\t"
        "ds_write_b64 v114, v[98:99] offset:0\n\t"
        "s_add_u32 s52, s52, 18432\n\t"
        "buffer_load_dwordx4 v[68:71], v108, %[rq], s52 offen\n\t"
        "v_mfma_f32_16x16x32_bf16 v[44:47], a[84:87], v[52:55], v[44:47]\n\t"
        "s_add_u32 s52, s52, 18432\n\t"
        "buffer_load_dwordx4 v[72:75], v108, %[rq], s52 offen\n\t"
        "s_lshl_b32 s40, s61, 9\n\t"
        "v_add_u32 v124, s40, v121\n\t"
        "ds_read_b128 v[100:103], v124\n\t"
        "ds_read_b128 v[104:107], v124 offset:4608\n\t"
        "ds_read_b128 v[52:55], v118 offset:40960\n\t"
        "s_add_u32 m0, s47, 36864\n\t"
        "s_lshl_b32 s40, s62, 7\n\t"
        "buffer_load_dwordx4 v109, %[rx], s40 offen lds\n\t"
        "s_add_u32 m0, s47, 45056\n\t"
        "s_lshl_b32 s40, s62, 7\n\t"
        "buffer_load_dwordx4 v110, %[rx], s40 offen lds\n\t"
        "s_waitcnt lgkmcnt(8)\n\t"
        "v_mfma_f32_16x16x32_bf16 v[16:19], a[88:91], v[56:59], v[16:19]\n\t"
        "v_mfma_f32_16x16x32_bf16 v[20:23], a[92:95], v[56:59], v[20:23]\n\t"
        "ds_read_b128 v[56:59], v118 offset:43008\n\t"
        "s_waitcnt lgkmcnt(4)\n\t"
        "s_waitcnt vmcnt(12)\n\t"
        "L_join6_%=:\n\t"
        "s_barrier\n\t"
        "; interval 7\n\t"
        "ds_read_b128 a[96:99], v119 offset:0\n\t"
        "ds_read_b128 a[100:103], v120 offset:0\n\t"
        "s_waitcnt vmcnt(8)\n\t"
        "v_or3_b32 v123, v122, v76, v77\n\t"
        "v_mfma_f32_16x16x32_bf16 v[24:27], a[88:91], v[48:51], v[24:27]\n\t"
        "v_or3_b32 v124, v78, v79, v80\n\t"
        "v_or3_b32 v123, v123, v81, v82\n\t"
        "v_mfma_f32_16x16x32_bf16 v[28:31], a[92:95], v[48:51], v[28:31]\n\t"
        "v_or3_b32 v124, v124, v83, v84\n\t"
        "ds_read_b128 v[48:51], v115 offset:0\n\t"
        "v_or3_b32 v123, v123, v85, v86\n\t"
        "v_or3_b32 v124, v124, v87, v88\n\t"
        "s_waitcnt lgkmcnt(4)\n\t"
        "v_mfma_f32_16x16x32_bf16 v[32:35], a[88:91], v[52:55], v[32:35]\n\t"
        "v_or3_b32 v123, v123, v89, v90\n\t"
        "v_or3_b32 v123, v123, v124, v91\n\t"
        "v_mfma_f32_16x16x32_bf16 v[36:39], a[92:95], v[52:55], v[36:39]\n\t"
        "v_cmp_lt_u32 vcc, 0x7f, v123\n\t"
        "s_cbranch_vccnz L_slow7_%=\n\t"
        "ds_read_b128 v[52:55], v115 offset:2048\n\t"
        "v_fma_mix_f32 v76, v76, v100, v104 op_sel_hi:[1,0,0]\n\t"
        "v_fma_mix_f32 v77, v77, v101, v105 op_sel_hi:[1,0,0]\n\t"
        "v_fma_mix_f32 v78, v78, v102, v106 op_sel_hi:[1,0,0]\n\t"
        "ds_read_b128 a[104:107], v119 offset:8192\n\t"
        "ds_read_b128 a[108:111], v120 offset:8192\n\t"
        "v_fma_mix_f32 v79, v79, v103, v107 op_sel_hi:[1,0,0]\n\t"
        "v_fma_mix_f32 v80, v80, v100, v104 op_sel_hi:[1,0,0]\n\t"
        "s_waitcnt lgkmcnt(6)\n\t"
        "v_mfma_f32_16x16x32_bf16 v[40:43], a[88:91], v[56:59], v[40:43]\n\t"
        "v_fma_mix_f32 v81, v81, v101, v105 op_sel_hi:[1,0,0]\n\t"
        "v_fma_mix_f32 v82, v82, v102, v106 op_sel_hi:[1,0,0]\n\t"
        "v_mfma_f32_16x16x32_bf16 v[44:47], a[92:95], v[56:59], v[44:47]\n\t"
        "v_fma_mix_f32 v83, v83, v103, v107 op_sel_hi:[1,0,0]\n\t"
        "v_cvt_pk_bf16_f32 v92, v76, v80\n\t"
        "ds_read_b128 v[56:59], v115 offset:4096\n\t"
        "v_cvt_pk_bf16_f32 v94, v77, v81\n\t"
        "v_cvt_pk_bf16_f32 v96, v78, v82\n\t"
        "s_waitcnt lgkmcnt(5)\n\t"
        "s_waitcnt lgkmcnt(4)\n\t"
        "v_mfma_f32_16x16x32_bf16 v[16:19], a[96:99], v[48:51], v[16:19]\n\t"
        "v_cvt_pk_bf16_f32 v98, v79, v83\n\t"
        "v_fma_mix_f32 v84, v84, v100, v104 op_sel_hi:[1,0,0]\n\t"
        "v_mfma_f32_16x16x32_bf16 v[20:23], a[100:103], v[48:51], v[20:23]\n\t"
        "v_fma_mix_f32 v85, v85, v101, v105 op_sel_hi:[1,0,0]\n\t"
        "v_fma_mix_f32 v86, v86, v102, v106 op_sel_hi:[1,0,0]\n\t"
        "ds_read_b128 v[48:51], v115 offset:6144\n\t"
        "v_fma_mix_f32 v87, v87, v103, v107 op_sel_hi:[1,0,0]\n\t"
        "v_fma_mix_f32 v88, v88, v100, v104 op_sel_hi:[1,0,0]\n\t"
        "s_waitcnt lgkmcnt(4)\n\t"
        "v_mfma_f32_16x16x32_bf16 v[24:27], a[96:99], v[52:55], v[24:27]\n\t"
        "v_fma_mix_f32 v89, v89, v101, v105 op_sel_hi:[1,0,0]\n\t"
        "v_fma_mix_f32 v90, v90, v102, v106 op_sel_hi:[1,0,0]\n\t"
        "v_fma_mix_f32 v91, v91, v103, v107 op_sel_hi:[1,0,0]\n\t"
        "v_mfma_f32_16x16x32_bf16 v[28:31], a[100:103], v[52:55], v[28:31]\n\t"
        "v_cvt_pk_bf16_f32 v93, v84, v88\n\t"
        "ds_write_b64 v111, v[92:93] offset:16384\n\t"
        "ds_read_b128 v[52:55], v116 offset:0\n\t"
        "v_cvt_pk_bf16_f32 v95, v85, v89\n\t"
        "ds_write_b64 v112, v[94:95] offset:16384\n\t"
        "s_waitcnt lgkmcnt(4)\n\t"
        "v_mfma_f32_16x16x32_bf16 v[32:35], a[96:99], v[56:59], v[32:35]\n\t"
        "v_cvt_pk_bf16_f32 v97, v86, v90\n\t"
        "ds_write_b64 v113, v[96:97] offset:16384\n\t"
        "v_mfma_f32_16x16x32_bf16 v[36:39], a[100:103], v[56:59], v[36:39]\n\t"
        "v_cvt_pk_bf16_f32 v99, v87, v91\n\t"
        "ds_write_b64 v114, v[98:99] offset:16384\n\t"
        "ds_read_b128 v[56:59], v116 offset:2048\n\t"
        "s_lshl_b32 s40, s62, 9\n\t"
        "v_add_u32 v124, s40, v121\n\t"
        "ds_read_b128 v[100:103], v124\n\t"
        "ds_read_b128 v[104:107], v124 offset:4608\n\t"
        "v_accvgpr_read_b32 v109, a127\n\t"
        "s_waitcnt lgkmcnt(8)\n\t"
        "v_mfma_f32_16x16x32_bf16 v[40:43], a[96:99], v[48:51], v[40:43]\n\t"
        "s_add_u32 s40, s45, 8\n\t"
        "s_add_u32 s51, %[start], s40\n\t"
        "s_cmp_ge_u32 s51, %[nblk]\n\t"
        "s_cselect_b32 s43, %[nblk], 0\n\t"
        "s_sub_u32 s51, s51, s43\n\t"
        "s_add_u32 s51, s51, %[blk0]\n\t"
        "s_mul_i32 s51, s51, 18432\n\t"
        "s_mov_b32 s41, 55296\n\t"
        "s_cmp_lg_u32 s45, 0\n\t"
        "s_cselect_b32 s41, 73728, s41\n\t"
        "s_add_u32 s50, s41, s53\n\t"
        "s_cmp_ge_u32 s44, 2\n\t"
        "s_cbranch_scc1 L_x2s1_%=\n\t"
        "s_add_u32 m0, s50, 16384\n\t"
        "s_lshl_b32 s40, s62, 7\n\t"
        "s_add_u32 s40, s40, s51\n\t"
        "buffer_load_dwordx4 v109, %[rx], s40 offen lds\n\t"
        "L_x2s1_%=:\n\t"
        "v_mfma_f32_16x16x32_bf16 v[44:47], a[100:103], v[48:51], v[44:47]\n\t"
        "s_add_u32 m0, s50, 0\n\t"
        "s_cmp_ge_u32 s44, 2\n\t"
        "s_cselect_b32 s40, s55, s54\n\t"
        "s_lshl_b32 s40, s40, 7\n\t"
        "s_add_u32 s40, s40, s51\n\t"
        "buffer_load_dwordx4 v109, %[rx], s40 offen lds\n\t"
        "s_add_u32 m0, s50, 4096\n\t"
        "s_cmp_ge_u32 s44, 2\n\t"
        "s_cselect_b32 s40, s57, s56\n\t"
        "s_lshl_b32 s40, s40, 7\n\t"
        "s_add_u32 s40, s40, s51\n\t"
        "buffer_load_dwordx4 v109, %[rx], s40 offen lds\n\t"
        "ds_read_b128 v[48:51], v116 offset:4096\n\t"
        "s_add_u32 m0, s50, 8192\n\t"
        "s_cmp_ge_u32 s44, 2\n\t"
        "s_cselect_b32 s40, s59, s58\n\t"
        "s_lshl_b32 s40, s40, 7\n\t"
        "s_add_u32 s40, s40, s51\n\t"
        "buffer_load_dwordx4 v109, %[rx], s40 offen lds\n\t"
        "s_add_u32 m0, s50, 12288\n\t"
        "s_cmp_ge_u32 s44, 2\n\t"
        "s_cselect_b32 s40, s61, s60\n\t"
        "s_lshl_b32 s40, s40, 7\n\t"
        "s_add_u32 s40, s40, s51\n\t"
        "buffer_load_dwordx4 v109, %[rx], s40 offen lds\n\t"
        "s_waitcnt lgkmcnt(7)\n\t"
        "v_mfma_f32_16x16x32_bf16 v[16:19], a[104:107], v[52:55], v[16:19]\n\t"
        "v_mfma_f32_16x16x32_bf16 v[20:23], a[108:111], v[52:55], v[20:23]\n\t"
        "ds_read_b128 v[52:55], v116 offset:6144\n\t"
        "s_waitcnt lgkmcnt(5)\n\t"
        "s_waitcnt vmcnt(10)\n\t"
        "L_join7_%=:\n\t"
        "s_barrier\n\t"
        "; interval 8\n\t"
        "ds_read_b128 a[112:115], v119 offset:16384\n\t"
        "ds_read_b128 a[116:119], v120 offset:16384\n\t"
        "s_waitcnt vmcnt(6)\n\t"
        "v_or3_b32 v123, v122, v60, v61\n\t"
        "s_waitcnt lgkmcnt(6)\n\t"
        "v_mfma_f32_16x16x32_bf16 v[24:27], a[104:107], v[56:59], v[24:27]\n\t"
        "v_or3_b32 v124, v62, v63, v64\n\t"
        "v_or3_b32 v123, v123, v65, v66\n\t"
        "v_mfma_f32_16x16x32_bf16 v[28:31], a[108:111], v[56:59], v[28:31]\n\t"
        "v_or3_b32 v124, v124, v67, v68\n\t"
        "ds_read_b128 v[56:59], v115 offset:18432\n\t"
        "v_or3_b32 v123, v123, v69, v70\n\t"
        "v_or3_b32 v124, v124, v71, v72\n\t"
        "s_waitcnt lgkmcnt(4)\n\t"
        "v_mfma_f32_16x16x32_bf16 v[32:35], a[104:107], v[48:51], v[32:35]\n\t"
        "v_or3_b32 v123, v123, v73, v74\n\t"
        "v_or3_b32 v123, v123, v124, v75\n\t"
        "v_mfma_f32_16x16x32_bf16 v[36:39], a[108:111], v[48:51], v[36:39]\n\t"
        "v_cmp_lt_u32 vcc, 0x7f, v123\n\t"
        "s_cbranch_vccnz L_slow8_%=\n\t"
        "ds_read_b128 v[48:51], v115 offset:20480\n\t"
        "v_fma_mix_f32 v60, v60, v100, v104 op_sel_hi:[1,0,0]\n\t"
        "v_fma_mix_f32 v61, v61, v101, v105 op_sel_hi:[1,0,0]\n\t"
        "ds_read_b128 a[120:123], v119 offset:24576\n\t"
        "ds_read_b128 a[124:127], v120 offset:24576\n\t"
        "v_fma_mix_f32 v62, v62, v102, v106 op_sel_hi:[1,0,0]\n\t"
        "v_fma_mix_f32 v63, v63, v103, v107 op_sel_hi:[1,0,0]\n\t"
        "s_waitcnt lgkmcnt(6)\n\t"
        "v_mfma_f32_16x16x32_bf16 v[40:43], a[104:107], v[52:55], v[40:43]\n\t"
        "v_fma_mix_f32 v64, v64, v100, v104 op_sel_hi:[1,0,0]\n\t"
        "v_mfma_f32_16x16x32_bf16 v[44:47], a[108:111], v[52:55], v[44:47]\n\t"
        "v_fma_mix_f32 v65, v65, v101, v105 op_sel_hi:[1,0,0]\n\t"
        "v_fma_mix_f32 v66, v66, v102, v106 op_sel_hi:[1,0,0]\n\t"
        "ds_read_b128 v[52:55], v115 offset:22528\n\t"
        "v_fma_mix_f32 v67, v67, v103, v107 op_sel_hi:[1,0,0]\n\t"
        "v_cvt_pk_bf16_f32 v92, v60, v64\n\t"
        "s_waitcnt lgkmcnt(5)\n\t"
        "s_waitcnt lgkmcnt(4)\n\t"
        "v_mfma_f32_16x16x32_bf16 v[16:19], a[112:115], v[56:59], v[16:19]\n\t"
        "v_cvt_pk_bf16_f32 v94, v61, v65\n\t"
        "v_mfma_f32_16x16x32_bf16 v[20:23], a[116:119], v[56:59], v[20:23]\n\t"
        "v_cvt_pk_bf16_f32 v96, v62, v66\n\t"
        "v_cvt_pk_bf16_f32 v98, v63, v67\n\t"
        "ds_read_b128 v[56:59], v115 offset:24576\n\t"
        "v_fma_mix_f32 v68, v68, v100, v104 op_sel_hi:[1,0,0]\n\t"
        "v_fma_mix_f32 v69, v69, v101, v105 op_sel_hi:[1,0,0]\n\t"
        "s_waitcnt lgkmcnt(4)\n\t"
        "v_mfma_f32_16x16x32_bf16 v[24:27], a[112:115], v[48:51], v[24:27]\n\t"
        "v_fma_mix_f32 v70, v70, v102, v106 op_sel_hi:[1,0,0]\n\t"
        "v_mfma_f32_16x16x32_bf16 v[28:31], a[116:119], v[48:51], v[28:31]\n\t"
        "v_fma_mix_f32 v71, v71, v103, v107 op_sel_hi:[1,0,0]\n\t"
        "v_fma_mix_f32 v72, v72, v100, v104 op_sel_hi:[1,0,0]\n\t"
        "ds_read_b128 v[48:51], v116 offset:18432\n\t"
        "v_fma_mix_f32 v73, v73, v101, v105 op_sel_hi:[1,0,0]\n\t"
        "v_fma_mix_f32 v74, v74, v102, v106 op_sel_hi:[1,0,0]\n\t"
        "s_waitcnt lgkmcnt(2)\n\t"
        "v_mfma_f32_16x16x32_bf16 v[32:35], a[112:115], v[52:55], v[32:35]\n\t"
        "v_fma_mix_f32 v75, v75, v103, v107 op_sel_hi:[1,0,0]\n\t"
        "v_mfma_f32_16x16x32_bf16 v[36:39], a[116:119], v[52:55], v[36:39]\n\t"
        "v_cvt_pk_bf16_f32 v93, v68, v72\n\t"
        "ds_write_b64 v111, v[92:93] offset:0\n\t"
        "ds_read_b128 v[52:55], v116 offset:20480\n\t"
        "v_cvt_pk_bf16_f32 v95, v69, v73\n\t"
        "ds_write_b64 v112, v[94:95] offset:0\n\t"
        "s_waitcnt lgkmcnt(4)\n\t"
        "v_mfma_f32_16x16x32_bf16 v[40:43], a[112:115], v[56:59], v[40:43]\n\t"
        "v_cvt_pk_bf16_f32 v97, v70, v74\n\t"
        "v_mfma_f32_16x16x32_bf16 v[44:47], a[116:119], v[56:59], v[44:47]\n\t"
        "ds_write_b64 v113, v[96:97] offset:0\n\t"
        "v_cvt_pk_bf16_f32 v99, v71, v75\n\t"
        "ds_read_b128 v[56:59], v116 offset:22528\n\t"
        "ds_write_b64 v114, v[98:99] offset:0\n\t"
        "s_waitcnt lgkmcnt(6)\n\t"
        "v_mfma_f32_16x16x32_bf16 v[16:19], a[120:123], v[48:51], v[16:19]\n\t"
        "v_mfma_f32_16x16x32_bf16 v[20:23], a[124:127], v[48:51], v[20:23]\n\t"
        "ds_read_b128 v[48:51], v116 offset:24576\n\t"
        "s_waitcnt lgkmcnt(1)\n\t"
        "s_waitcnt vmcnt(4)\n\t"
        "L_join8_%=:\n\t"
        "s_barrier\n\t"
        "; group 8 + phase 2\n\t"
        "ds_read_b128 v[0:3], v119 offset:0\n\t"
        "ds_read_b128 v[4:7], v120 offset:0\n\t"
        "v_mov_b32 v105, v109\n\t"
        "v_mfma_f32_16x16x32_bf16 v[24:27], a[120:123], v[52:55], v[24:27]\n\t"
        "s_add_u32 s40, s45, 10\n\t"
        "s_add_u32 s51, %[start], s40\n\t"
        "s_cmp_ge_u32 s51, %[nblk]\n\t"
        "s_cselect_b32 s43, %[nblk], 0\n\t"
        "s_sub_u32 s51, s51, s43\n\t"
        "s_add_u32 s51, s51, %[blk0]\n\t"
        "s_mul_i32 s51, s51, 18432\n\t"
        "s_mov_b32 s41, 92160\n\t"
        "s_cmp_lg_u32 s45, 0\n\t"
        "s_cselect_b32 s41, 0, s41\n\t"
        "s_add_u32 s50, s41, s53\n\t"
        "v_mfma_f32_16x16x32_bf16 v[28:31], a[124:127], v[52:55], v[28:31]\n\t"
        "s_cmp_ge_u32 s44, 2\n\t"
        "s_cbranch_scc1 L_x2s3_%=\n\t"
        "s_add_u32 m0, s50, 16384\n\t"
        "s_lshl_b32 s40, s62, 7\n\t"
        "s_add_u32 s40, s40, s51\n\t"
        "buffer_load_dwordx4 v105, %[rx], s40 offen lds\n\t"
        "L_x2s3_%=:\n\t"
        "ds_read_b128 v[52:55], v115 offset:36864\n\t"
        "v_mfma_f32_16x16x32_bf16 v[32:35], a[120:123], v[56:59], v[32:35]\n\t"
        "s_add_u32 m0, s50, 0\n\t"
        "s_cmp_ge_u32 s44, 2\n\t"
        "s_cselect_b32 s40, s55, s54\n\t"
        "s_lshl_b32 s40, s40, 7\n\t"
        "s_add_u32 s40, s40, s51\n\t"
        "buffer_load_dwordx4 v105, %[rx], s40 offen lds\n\t"
        "v_mfma_f32_16x16x32_bf16 v[36:39], a[124:127], v[56:59], v[36:39]\n\t"
        "s_add_u32 m0, s50, 4096\n\t"
        "s_cmp_ge_u32 s44, 2\n\t"
        "s_cselect_b32 s40, s57, s56\n\t"
        "s_lshl_b32 s40, s40, 7\n\t"
        "s_add_u32 s40, s40, s51\n\t"
        "buffer_load_dwordx4 v105, %[rx], s40 offen lds\n\t"
        "ds_read_b128 v[56:59], v115 offset:38912\n\t"
        "s_add_u32 m0, s50, 8192\n\t"
        "s_cmp_ge_u32 s44, 2\n\t"
        "s_cselect_b32 s40, s59, s58\n\t"
        "s_lshl_b32 s40, s40, 7\n\t"
        "s_add_u32 s40, s40, s51\n\t"
        "buffer_load_dwordx4 v105, %[rx], s40 offen lds\n\t"
        "ds_read_b128 v[8:11], v119 offset:8192\n\t"
        "ds_read_b128 v[12:15], v120 offset:8192\n\t"
        "s_waitcnt lgkmcnt(6)\n\t"
        "v_mfma_f32_16x16x32_bf16 v[40:43], a[120:123], v[48:51], v[40:43]\n\t"
        "s_add_u32 m0, s50, 12288\n\t"
        "s_cmp_ge_u32 s44, 2\n\t"
        "s_cselect_b32 s40, s61, s60\n\t"
        "s_lshl_b32 s40, s40, 7\n\t"
        "s_add_u32 s40, s40, s51\n\t"
        "buffer_load_dwordx4 v105, %[rx], s40 offen lds\n\t"
        "v_mfma_f32_16x16x32_bf16 v[44:47], a[124:127], v[48:51], v[44:47]\n\t"
        "v_and_b32 v100, 15, %[lane]\n\t"
        "ds_read_b128 v[48:51], v115 offset:40960\n\t"
        "v_lshrrev_b32 v101, 4, %[lane]\n\t"
        "s_waitcnt lgkmcnt(5)\n\t"
        "s_waitcnt lgkmcnt(4)\n\t"
        "v_mfma_f32_16x16x32_bf16 v[16:19], v[0:3], v[52:55], v[16:19]\n\t"
        "v_mfma_f32_16x16x32_bf16 v[20:23], v[4:7], v[52:55], v[20:23]\n\t"
        "s_lshl_b32 s42, s44, 5\n\t"
        "ds_read_b128 v[52:55], v115 offset:43008\n\t"
        "s_add_u32 s42, s42, %[cgb]\n\t"
        "s_waitcnt lgkmcnt(4)\n\t"
        "v_mfma_f32_16x16x32_bf16 v[24:27], v[0:3], v[56:59], v[24:27]\n\t"
        "v_mfma_f32_16x16x32_bf16 v[28:31], v[4:7], v[56:59], v[28:31]\n\t"
        "s_lshl_b32 s42, s42, 1\n\t"
        "ds_read_b128 v[56:59], v116 offset:36864\n\t"
        "v_mul_u32_u24 v104, 9216, v100\n\t"
        "s_waitcnt lgkmcnt(2)\n\t"
        "v_mfma_f32_16x16x32_bf16 v[32:35], v[0:3], v[48:51], v[32:35]\n\t"
        "v_lshl_add_u32 v104, v101, 4, v104\n\t"
        "v_mfma_f32_16x16x32_bf16 v[36:39], v[4:7], v[48:51], v[36:39]\n\t"
        "ds_read_b128 v[48:51], v116 offset:38912\n\t"
        "v_add_u32 v104, s42, v104\n\t"
        "s_waitcnt lgkmcnt(2)\n\t"
        "v_mfma_f32_16x16x32_bf16 v[40:43], v[0:3], v[52:55], v[40:43]\n\t"
        "s_lshl_b32 s42, s45, 13\n\t"
        "v_mfma_f32_16x16x32_bf16 v[44:47], v[4:7], v[52:55], v[44:47]\n\t"
        "v_subrev_u32 v106, s42, v115\n\t"
        "ds_read_b128 v[52:55], v116 offset:40960\n\t"
        "s_waitcnt lgkmcnt(2)\n\t"
        "v_mfma_f32_16x16x32_bf16 v[16:19], v[8:11], v[56:59], v[16:19]\n\t"
        "v_xor_b32 v107, 64, v106\n\t"
        "v_mfma_f32_16x16x32_bf16 v[20:23], v[12:15], v[56:59], v[20:23]\n\t"
        "s_cmp_lg_u32 s45, 0\n\t"
        "s_mov_b32 s41, 55296\n\t"
        "s_cselect_b32 s41, 73728, s41\n\t"
        "v_add_u32 v117, s41, v106\n\t"
        "v_add_u32 v118, s41, v107\n\t"
        "ds_read_b128 v[56:59], v116 offset:43008\n\t"
        "s_waitcnt vmcnt(4)\n\t"
        "s_waitcnt lgkmcnt(0)\n\t"
        "s_barrier\n\t"
        "ds_read_b128 v[60:63], v117 offset:0\n\t"
        "v_mfma_f32_16x16x32_bf16 v[24:27], v[8:11], v[48:51], v[24:27]\n\t"
        "ds_read_b128 v[64:67], v118 offset:0\n\t"
        "v_mfma_f32_16x16x32_bf16 v[28:31], v[12:15], v[48:51], v[28:31]\n\t"
        "ds_read_b128 v[68:71], v117 offset:2048\n\t"
        "v_mfma_f32_16x16x32_bf16 v[32:35], v[8:11], v[52:55], v[32:35]\n\t"
        "ds_read_b128 v[72:75], v118 offset:2048\n\t"
        "v_mfma_f32_16x16x32_bf16 v[36:39], v[12:15], v[52:55], v[36:39]\n\t"
        "v_mfma_f32_16x16x32_bf16 v[40:43], v[8:11], v[56:59], v[40:43]\n\t"
        "v_mfma_f32_16x16x32_bf16 v[44:47], v[12:15], v[56:59], v[44:47]\n\t"
        "s_waitcnt lgkmcnt(3)\n\t"
        "v_mfma_f32_16x16x32_bf16 v[84:87], a[0:3], v[60:63], 0\n\t"
        "v_mfma_f32_16x16x32_bf16 v[88:91], a[4:7], v[60:63], 0\n\t"
        "ds_read_b128 v[60:63], v117 offset:4096\n\t"
        "s_waitcnt lgkmcnt(3)\n\t"
        "v_mfma_f32_16x16x32_bf16 v[84:87], a[8:11], v[64:67], v[84:87]\n\t"
        "s_cmp_lg_u32 s45, 0\n\t"
        "v_mfma_f32_16x16x32_bf16 v[88:91], a[12:15], v[64:67], v[88:91]\n\t"
        "ds_read_b128 v[64:67], v118 offset:4096\n\t"
        "s_mov_b32 s41, 92160\n\t"
        "s_waitcnt lgkmcnt(3)\n\t"
        "v_mfma_f32_16x16x32_bf16 v[84:87], a[16:19], v[68:71], v[84:87]\n\t"
        "s_cselect_b32 s41, 0, s41\n\t"
        "v_mfma_f32_16x16x32_bf16 v[88:91], a[20:23], v[68:71], v[88:91]\n\t"
        "ds_read_b128 v[68:71], v117 offset:6144\n\t"
        "v_add_u32 v115, s41, v106\n\t"
        "s_waitcnt lgkmcnt(3)\n\t"
        "v_mfma_f32_16x16x32_bf16 v[84:87], a[24:27], v[72:75], v[84:87]\n\t"
        "v_add_u32 v116, s41, v107\n\t"
        "v_mfma_f32_16x16x32_bf16 v[88:91], a[28:31], v[72:75], v[88:91]\n\t"
        "ds_read_b128 v[72:75], v118 offset:6144\n\t"
        "s_add_u32 s40, s45, 12\n\t"
        "s_add_u32 s51, %[start], s40\n\t"
        "s_cmp_ge_u32 s51, %[nblk]\n\t"
        "s_cselect_b32 s43, %[nblk], 0\n\t"
        "s_sub_u32 s51, s51, s43\n\t"
        "s_add_u32 s51, s51, %[blk0]\n\t"
        "s_mul_i32 s51, s51, 18432\n\t"
        "s_mov_b32 s41, 18432\n\t"
        "s_cmp_lg_u32 s45, 0\n\t"
        "s_cselect_b32 s41, 36864, s41\n\t"
        "s_add_u32 s50, s41, s53\n\t"
        "s_waitcnt lgkmcnt(3)\n\t"
        "v_mfma_f32_16x16x32_bf16 v[84:87], a[32:35], v[60:63], v[84:87]\n\t"
        "s_cmp_ge_u32 s44, 2\n\t"
        "s_cbranch_scc1 L_x2s4_%=\n\t"
        "s_add_u32 m0, s50, 16384\n\t"
        "s_lshl_b32 s40, s62, 7\n\t"
        "s_add_u32 s40, s40, s51\n\t"
        "buffer_load_dwordx4 v105, %[rx], s40 offen lds\n\t"
        "L_x2s4_%=:\n\t"
        "v_mfma_f32_16x16x32_bf16 v[88:91], a[36:39], v[60:63], v[88:91]\n\t"
        "ds_read_b128 v[60:63], v117 offset:8192\n\t"
        "s_add_u32 m0, s50, 0\n\t"
        "s_cmp_ge_u32 s44, 2\n\t"
        "s_cselect_b32 s40, s55, s54\n\t"
        "s_lshl_b32 s40, s40, 7\n\t"
        "s_add_u32 s40, s40, s51\n\t"
        "buffer_load_dwordx4 v105, %[rx], s40 offen lds\n\t"
        "s_waitcnt lgkmcnt(3)\n\t"
        "v_mfma_f32_16x16x32_bf16 v[84:87], a[40:43], v[64:67], v[84:87]\n\t"
        "s_add_u32 m0, s50, 4096\n\t"
        "s_cmp_ge_u32 s44, 2\n\t"
        "s_cselect_b32 s40, s57, s56\n\t"
        "s_lshl_b32 s40, s40, 7\n\t"
        "s_add_u32 s40, s40, s51\n\t"
        "buffer_load_dwordx4 v105, %[rx], s40 offen lds\n\t"
        "v_mfma_f32_16x16x32_bf16 v[88:91], a[44:47], v[64:67], v[88:91]\n\t"
        "ds_read_b128 v[64:67], v118 offset:8192\n\t"
        "s_add_u32 m0, s50, 8192\n\t"
        "s_cmp_ge_u32 s44, 2\n\t"
        "s_cselect_b32 s40, s59, s58\n\t"
        "s_lshl_b32 s40, s40, 7\n\t"
        "s_add_u32 s40, s40, s51\n\t"
        "buffer_load_dwordx4 v105, %[rx], s40 offen lds\n\t"
        "s_waitcnt lgkmcnt(3)\n\t"
        "v_mfma_f32_16x16x32_bf16 v[84:87], a[48:51], v[68:71], v[84:87]\n\t"
        "s_add_u32 m0, s50, 12288\n\t"
        "s_cmp_ge_u32 s44, 2\n\t"
        "s_cselect_b32 s40, s61, s60\n\t"
        "s_lshl_b32 s40, s40, 7\n\t"
        "s_add_u32 s40, s40, s51\n\t"
        "buffer_load_dwordx4 v105, %[rx], s40 offen lds\n\t"
        "v_mfma_f32_16x16x32_bf16 v[88:91], a[52:55], v[68:71], v[88:91]\n\t"
        "ds_read_b128 v[68:71], v117 offset:10240\n\t"
        "s_lshl_b32 s40, s45, 2\n\t"
        "s_add_u32 s40, s40, 0\n\t"
        "s_add_u32 s46, %[start], s40\n\t"
        "s_cmp_ge_u32 s46, %[nblk]\n\t"
        "s_cselect_b32 s43, %[nblk], 0\n\t"
        "s_sub_u32 s46, s46, s43\n\t"
        "s_add_u32 s46, s46, %[blk0]\n\t"
        "s_mul_i32 s46, s46, 147456\n\t"
        "s_waitcnt lgkmcnt(3)\n\t"
        "v_mfma_f32_16x16x32_bf16 v[84:87], a[56:59], v[72:75], v[84:87]\n\t"
        "v_cvt_pk_bf16_f32 v100, v16, v17\n\t"
        "v_mfma_f32_16x16x32_bf16 v[88:91], a[60:63], v[72:75], v[88:91]\n\t"
        "ds_read_b128 v[72:75], v118 offset:10240\n\t"
        "v_cvt_pk_bf16_f32 v101, v18, v19\n\t"
        "s_waitcnt lgkmcnt(3)\n\t"
        "v_mfma_f32_16x16x32_bf16 v[84:87], a[64:67], v[60:63], v[84:87]\n\t"
        "v_cvt_pk_bf16_f32 v102, v20, v21\n\t"
        "v_mfma_f32_16x16x32_bf16 v[88:91], a[68:71], v[60:63], v[88:91]\n\t"
        "ds_read_b128 v[60:63], v117 offset:12288\n\t"
        "v_cvt_pk_bf16_f32 v103, v22, v23\n\t"
        "s_waitcnt lgkmcnt(3)\n\t"
        "v_mfma_f32_16x16x32_bf16 v[84:87], a[72:75], v[64:67], v[84:87]\n\t"
        "buffer_store_dwordx4 v[100:103], v104, %[ro], s46 offen sc1\n\t"
        "v_mfma_f32_16x16x32_bf16 v[88:91], a[76:79], v[64:67], v[88:91]\n\t"
        "ds_read_b128 v[64:67], v118 offset:12288\n\t"
        "s_waitcnt lgkmcnt(3)\n\t"
        "v_mfma_f32_16x16x32_bf16 v[84:87], a[80:83], v[68:71], v[84:87]\n\t"
        "v_mfma_f32_16x16x32_bf16 v[88:91], a[84:87], v[68:71], v[88:91]\n\t"
        "ds_read_b128 v[68:71], v117 offset:14336\n\t"
        "s_waitcnt lgkmcnt(3)\n\t"
        "v_mfma_f32_16x16x32_bf16 v[84:87], a[88:91], v[72:75], v[84:87]\n\t"
        "v_mfma_f32_16x16x32_bf16 v[88:91], a[92:95], v[72:75], v[88:91]\n\t"
        "ds_read_b128 v[72:75], v118 offset:14336\n\t"
        "s_waitcnt lgkmcnt(3)\n\t"
        "v_mfma_f32_16x16x32_bf16 v[84:87], a[96:99], v[60:63], v[84:87]\n\t"
        "v_mfma_f32_16x16x32_bf16 v[88:91], a[100:103], v[60:63], v[88:91]\n\t"
        "ds_read_b128 v[60:63], v117 offset:16384\n\t"
        "s_waitcnt lgkmcnt(3)\n\t"
        "v_mfma_f32_16x16x32_bf16 v[84:87], a[104:107], v[64:67], v[84:87]\n\t"
        "v_mfma_f32_16x16x32_bf16 v[88:91], a[108:111], v[64:67], v[88:91]\n\t"
        "ds_read_b128 v[64:67], v118 offset:16384\n\t"
        "s_waitcnt lgkmcnt(3)\n\t"
        "v_mfma_f32_16x16x32_bf16 v[84:87], a[112:115], v[68:71], v[84:87]\n\t"
        "v_mfma_f32_16x16x32_bf16 v[88:91], a[116:119], v[68:71], v[88:91]\n\t"
        "s_waitcnt vmcnt(5)\n\t"
        "s_waitcnt lgkmcnt(0)\n\t"
        "s_barrier\n\t"
        "ds_read_b128 v[68:71], v115 offset:0\n\t"
        "v_mfma_f32_16x16x32_bf16 v[84:87], a[120:123], v[72:75], v[84:87]\n\t"
        "v_mfma_f32_16x16x32_bf16 v[88:91], a[124:127], v[72:75], v[88:91]\n\t"
        "ds_read_b128 v[72:75], v116 offset:0\n\t"
        "v_mfma_f32_16x16x32_bf16 v[84:87], v[0:3], v[60:63], v[84:87]\n\t"
        "v_mfma_f32_16x16x32_bf16 v[88:91], v[4:7], v[60:63], v[88:91]\n\t"
        "ds_read_b128 v[60:63], v115 offset:2048\n\t"
        "v_mfma_f32_16x16x32_bf16 v[84:87], v[8:11], v[64:67], v[84:87]\n\t"
        "v_mfma_f32_16x16x32_bf16 v[88:91], v[12:15], v[64:67], v[88:91]\n\t"
        "ds_read_b128 v[64:67], v116 offset:2048\n\t"
        "s_waitcnt lgkmcnt(3)\n\t"
        "v_mfma_f32_16x16x32_bf16 v[92:95], a[0:3], v[68:71], 0\n\t"
        "v_mfma_f32_16x16x32_bf16 v[96:99], a[4:7], v[68:71], 0\n\t"
        "ds_read_b128 v[68:71], v115 offset:4096\n\t"
        "s_waitcnt lgkmcnt(3)\n\t"
        "v_mfma_f32_16x16x32_bf16 v[92:95], a[8:11], v[72:75], v[92:95]\n\t"
        "s_cmp_lg_u32 s45, 0\n\t"
        "v_mfma_f32_16x16x32_bf16 v[96:99], a[12:15], v[72:75], v[96:99]\n\t"
        "ds_read_b128 v[72:75], v116 offset:4096\n\t"
        "s_mov_b32 s41, 18432\n\t"
        "s_waitcnt lgkmcnt(3)\n\t"
        "v_mfma_f32_16x16x32_bf16 v[92:95], a[16:19], v[60:63], v[92:95]\n\t"
        "s_cselect_b32 s41, 36864, s41\n\t"
        "v_mfma_f32_16x16x32_bf16 v[96:99], a[20:23], v[60:63], v[96:99]\n\t"
        "ds_read_b128 v[60:63], v115 offset:6144\n\t"
        "v_add_u32 v117, s41, v106\n\t"
        "s_waitcnt lgkmcnt(3)\n\t"
        "v_mfma_f32_16x16x32_bf16 v[92:95], a[24:27], v[64:67], v[92:95]\n\t"
        "v_add_u32 v118, s41, v107\n\t"
        "v_mfma_f32_16x16x32_bf16 v[96:99], a[28:31], v[64:67], v[96:99]\n\t"
        "ds_read_b128 v[64:67], v116 offset:6144\n\t"
        "s_add_u32 s40, s45, 14\n\t"
        "s_add_u32 s51, %[start], s40\n\t"
        "s_cmp_ge_u32 s51, %[nblk]\n\t"
        "s_cselect_b32 s43, %[nblk], 0\n\t"
        "s_sub_u32 s51, s51, s43\n\t"
        "s_add_u32 s51, s51, %[blk0]\n\t"
        "s_mul_i32 s51, s51, 18432\n\t"
        "s_mov_b32 s41, 55296\n\t"
        "s_cmp_lg_u32 s45, 0\n\t"
        "s_cselect_b32 s41, 73728, s41\n\t"
        "s_add_u32 s50, s41, s53\n\t"
        "s_waitcnt lgkmcnt(3)\n\t"
        "v_mfma_f32_16x16x32_bf16 v[92:95], a[32:35], v[68:71], v[92:95]\n\t"
        "s_cmp_ge_u32 s44, 2\n\t"
        "s_cbranch_scc1 L_x2s5_%=\n\t"
        "s_add_u32 m0, s50, 16384\n\t"
        "s_lshl_b32 s40, s62, 7\n\t"
        "s_add_u32 s40, s40, s51\n\t"
        "buffer_load_dwordx4 v105, %[rx], s40 offen lds\n\t"
        "L_x2s5_%=:\n\t"
        "v_mfma_f32_16x16x32_bf16 v[96:99], a[36:39], v[68:71], v[96:99]\n\t"
        "ds_read_b128 v[68:71], v115 offset:8192\n\t"
        "s_add_u32 m0, s50, 0\n\t"
        "s_cmp_ge_u32 s44, 2\n\t"
        "s_cselect_b32 s40, s55, s54\n\t"
        "s_lshl_b32 s40, s40, 7\n\t"
        "s_add_u32 s40, s40, s51\n\t"
        "buffer_load_dwordx4 v105, %[rx], s40 offen lds\n\t"
        "s_waitcnt lgkmcnt(3)\n\t"
        "v_mfma_f32_16x16x32_bf16 v[92:95], a[40:43], v[72:75], v[92:95]\n\t"
        "s_add_u32 m0, s50, 4096\n\t"
        "s_cmp_ge_u32 s44, 2\n\t"
        "s_cselect_b32 s40, s57, s56\n\t"
        "s_lshl_b32 s40, s40, 7\n\t"
        "s_add_u32 s40, s40, s51\n\t"
        "buffer_load_dwordx4 v105, %[rx], s40 offen lds\n\t"
        "v_mfma_f32_16x16x32_bf16 v[96:99], a[44:47], v[72:75], v[96:99]\n\t"
        "ds_read_b128 v[72:75], v116 offset:8192\n\t"
        "s_add_u32 m0, s50, 8192\n\t"
        "s_cmp_ge_u32 s44, 2\n\t"
        "s_cselect_b32 s40, s59, s58\n\t"
        "s_lshl_b32 s40, s40, 7\n\t"
        "s_add_u32 s40, s40, s51\n\t"
        "buffer_load_dwordx4 v105, %[rx], s40 offen lds\n\t"
        "s_waitcnt lgkmcnt(3)\n\t"
        "v_mfma_f32_16x16x32_bf16 v[92:95], a[48:51], v[60:63], v[92:95]\n\t"
        "s_add_u32 m0, s50, 12288\n\t"
        "s_cmp_ge_u32 s44, 2\n\t"
        "s_cselect_b32 s40, s61, s60\n\t"
        "s_lshl_b32 s40, s40, 7\n\t"
        "s_add_u32 s40, s40, s51\n\t"
        "buffer_load_dwordx4 v105, %[rx], s40 offen lds\n\t"
        "v_mfma_f32_16x16x32_bf16 v[96:99], a[52:55], v[60:63], v[96:99]\n\t"
        "ds_read_b128 v[60:63], v115 offset:10240\n\t"
        "s_add_u32 s40, s45, 8\n\t"
        "s_add_u32 s46, %[start], s40\n\t"
        "s_cmp_ge_u32 s46, %[nblk]\n\t"
        "s_cselect_b32 s43, %[nblk], 0\n\t"
        "s_sub_u32 s46, s46, s43\n\t"
        "s_add_u32 s46, s46, %[blk0]\n\t"
        "s_mul_i32 s46, s46, 147456\n\t"
        "s_waitcnt lgkmcnt(3)\n\t"
        "v_mfma_f32_16x16x32_bf16 v[92:95], a[56:59], v[64:67], v[92:95]\n\t"
        "v_cvt_pk_bf16_f32 v100, v84, v85\n\t"
        "v_mfma_f32_16x16x32_bf16 v[96:99], a[60:63], v[64:67], v[96:99]\n\t"
        "ds_read_b128 v[64:67], v116 offset:10240\n\t"
        "v_cvt_pk_bf16_f32 v101, v86, v87\n\t"
        "s_waitcnt lgkmcnt(3)\n\t"
        "v_mfma_f32_16x16x32_bf16 v[92:95], a[64:67], v[68:71], v[92:95]\n\t"
        "v_cvt_pk_bf16_f32 v102, v88, v89\n\t"
        "v_mfma_f32_16x16x32_bf16 v[96:99], a[68:71], v[68:71], v[96:99]\n\t"
        "ds_read_b128 v[68:71], v115 offset:12288\n\t"
        "v_cvt_pk_bf16_f32 v103, v90, v91\n\t"
        "s_waitcnt lgkmcnt(3)\n\t"
        "v_mfma_f32_16x16x32_bf16 v[92:95], a[72:75], v[72:75], v[92:95]\n\t"
        "buffer_store_dwordx4 v[100:103], v104, %[ro], s46 offen sc1\n\t"
        "v_mfma_f32_16x16x32_bf16 v[96:99], a[76:79], v[72:75], v[96:99]\n\t"
        "ds_read_b128 v[72:75], v116 offset:12288\n\t"
        "s_lshl_b32 s40, s45, 2\n\t"
        "s_add_u32 s40, s40, 1\n\t"
        "s_add_u32 s46, %[start], s40\n\t"
        "s_cmp_ge_u32 s46, %[nblk]\n\t"
        "s_cselect_b32 s43, %[nblk], 0\n\t"
        "s_sub_u32 s46, s46, s43\n\t"
        "s_add_u32 s46, s46, %[blk0]\n\t"
        "s_mul_i32 s46, s46, 147456\n\t"
        "s_waitcnt lgkmcnt(3)\n\t"
        "v_mfma_f32_16x16x32_bf16 v[92:95], a[80:83], v[60:63], v[92:95]\n\t"
        "v_cvt_pk_bf16_f32 v100, v24, v25\n\t"
        "v_mfma_f32_16x16x32_bf16 v[96:99], a[84:87], v[60:63], v[96:99]\n\t"
        "ds_read_b128 v[60:63], v115 offset:14336\n\t"
        "v_cvt_pk_bf16_f32 v101, v26, v27\n\t"
        "s_waitcnt lgkmcnt(3)\n\t"
        "v_mfma_f32_16x16x32_bf16 v[92:95], a[88:91], v[64:67], v[92:95]\n\t"
        "v_cvt_pk_bf16_f32 v102, v28, v29\n\t"
        "v_mfma_f32_16x16x32_bf16 v[96:99], a[92:95], v[64:67], v[96:99]\n\t"
        "ds_read_b128 v[64:67], v116 offset:14336\n\t"
        "v_cvt_pk_bf16_f32 v103, v30, v31\n\t"
        "s_waitcnt lgkmcnt(3)\n\t"
        "v_mfma_f32_16x16x32_bf16 v[92:95], a[96:99], v[68:71], v[92:95]\n\t"
        "buffer_store_dwordx4 v[100:103], v104, %[ro], s46 offen sc1\n\t"
        "v_mfma_f32_16x16x32_bf16 v[96:99], a[100:103], v[68:71], v[96:99]\n\t"
        "ds_read_b128 v[68:71], v115 offset:16384\n\t"
        "s_waitcnt lgkmcnt(3)\n\t"
        "v_mfma_f32_16x16x32_bf16 v[92:95], a[104:107], v[72:75], v[92:95]\n\t"
        "v_mfma_f32_16x16x32_bf16 v[96:99], a[108:111], v[72:75], v[96:99]\n\t"
        "ds_read_b128 v[72:75], v116 offset:16384\n\t"
        "s_waitcnt lgkmcnt(3)\n\t"
        "v_mfma_f32_16x16x32_bf16 v[92:95], a[112:115], v[60:63], v[92:95]\n\t"
        "v_mfma_f32_16x16x32_bf16 v[96:99], a[116:119], v[60:63], v[96:99]\n\t"
        "s_waitcnt vmcnt(7)\n\t"
        "s_waitcnt lgkmcnt(0)\n\t"
        "s_barrier\n\t"
        "ds_read_b128 v[60:63], v117 offset:0\n\t"
        "v_mfma_f32_16x16x32_bf16 v[92:95], a[120:123], v[64:67], v[92:95]\n\t"
        "v_mfma_f32_16x16x32_bf16 v[96:99], a[124:127], v[64:67], v[96:99]\n\t"
        "ds_read_b128 v[64:67], v118 offset:0\n\t"
        "v_mfma_f32_16x16x32_bf16 v[92:95], v[0:3], v[68:71], v[92:95]\n\t"
        "v_mfma_f32_16x16x32_bf16 v[96:99], v[4:7], v[68:71], v[96:99]\n\t"
        "ds_read_b128 v[68:71], v117 offset:2048\n\t"
        "v_mfma_f32_16x16x32_bf16 v[92:95], v[8:11], v[72:75], v[92:95]\n\t"
        "v_mfma_f32_16x16x32_bf16 v[96:99], v[12:15], v[72:75], v[96:99]\n\t"
        "ds_read_b128 v[72:75], v118 offset:2048\n\t"
        "s_waitcnt lgkmcnt(3)\n\t"
        "v_mfma_f32_16x16x32_bf16 v[84:87], a[0:3], v[60:63], 0\n\t"
        "v_mfma_f32_16x16x32_bf16 v[88:91], a[4:7], v[60:63], 0\n\t"
        "ds_read_b128 v[60:63], v117 offset:4096\n\t"
        "s_waitcnt lgkmcnt(3)\n\t"
        "v_mfma_f32_16x16x32_bf16 v[84:87], a[8:11], v[64:67], v[84:87]\n\t"
        "s_cmp_lg_u32 s45, 0\n\t"
        "v_mfma_f32_16x16x32_bf16 v[88:91], a[12:15], v[64:67], v[88:91]\n\t"
        "ds_read_b128 v[64:67], v118 offset:4096\n\t"
        "s_mov_b32 s41, 55296\n\t"
        "s_waitcnt lgkmcnt(3)\n\t"
        "v_mfma_f32_16x16x32_bf16 v[84:87], a[16:19], v[68:71], v[84:87]\n\t"
        "s_cselect_b32 s41, 73728, s41\n\t"
        "v_mfma_f32_16x16x32_bf16 v[88:91], a[20:23], v[68:71], v[88:91]\n\t"
        "ds_read_b128 v[68:71], v117 offset:6144\n\t"
        "v_add_u32 v115, s41, v106\n\t"
        "s_waitcnt lgkmcnt(3)\n\t"
        "v_mfma_f32_16x16x32_bf16 v[84:87], a[24:27], v[72:75], v[84:87]\n\t"
        "v_add_u32 v116, s41, v107\n\t"
        "v_mfma_f32_16x16x32_bf16 v[88:91], a[28:31], v[72:75], v[88:91]\n\t"
        "ds_read_b128 v[72:75], v118 offset:6144\n\t"
        "s_add_u32 s40, s45, 16\n\t"
        "s_add_u32 s51, %[start], s40\n\t"
        "s_cmp_ge_u32 s51, %[nblk]\n\t"
        "s_cselect_b32 s43, %[nblk], 0\n\t"
        "s_sub_u32 s51, s51, s43\n\t"
        "s_add_u32 s51, s51, %[blk0]\n\t"
        "s_mul_i32 s51, s51, 18432\n\t"
        "s_mov_b32 s41, 92160\n\t"
        "s_cmp_lg_u32 s45, 0\n\t"
        "s_cselect_b32 s41, 0, s41\n\t"
        "s_add_u32 s50, s41, s53\n\t"
        "s_waitcnt lgkmcnt(3)\n\t"
        "v_mfma_f32_16x16x32_bf16 v[84:87], a[32:35], v[60:63], v[84:87]\n\t"
        "s_cmp_ge_u32 s44, 2\n\t"
        "s_cbranch_scc1 L_x2s6_%=\n\t"
        "s_add_u32 m0, s50, 16384\n\t"
        "s_lshl_b32 s40, s62, 7\n\t"
        "s_add_u32 s40, s40, s51\n\t"
        "buffer_load_dwordx4 v105, %[rx], s40 offen lds\n\t"
        "L_x2s6_%=:\n\t"
        "v_mfma_f32_16x16x32_bf16 v[88:91], a[36:39], v[60:63], v[88:91]\n\t"
        "ds_read_b128 v[60:63], v117 offset:8192\n\t"
        "s_add_u32 m0, s50, 0\n\t"
        "s_cmp_ge_u32 s44, 2\n\t"
        "s_cselect_b32 s40, s55, s54\n\t"
        "s_lshl_b32 s40, s40, 7\n\t"
        "s_add_u32 s40, s40, s51\n\t"
        "buffer_load_dwordx4 v105, %[rx], s40 offen lds\n\t"
        "s_waitcnt lgkmcnt(3)\n\t"
        "v_mfma_f32_16x16x32_bf16 v[84:87], a[40:43], v[64:67], v[84:87]\n\t"
        "s_add_u32 m0, s50, 4096\n\t"
        "s_cmp_ge_u32 s44, 2\n\t"
        "s_cselect_b32 s40, s57, s56\n\t"
        "s_lshl_b32 s40, s40, 7\n\t"
        "s_add_u32 s40, s40, s51\n\t"
        "buffer_load_dwordx4 v105, %[rx], s40 offen lds\n\t"
        "v_mfma_f32_16x16x32_bf16 v[88:91], a[44:47], v[64:67], v[88:91]\n\t"
        "ds_read_b128 v[64:67], v118 offset:8192\n\t"
        "s_add_u32 m0, s50, 8192\n\t"
        "s_cmp_ge_u32 s44, 2\n\t"
        "s_cselect_b32 s40, s59, s58\n\t"
        "s_lshl_b32 s40, s40, 7\n\t"
        "s_add_u32 s40, s40, s51\n\t"
        "buffer_load_dwordx4 v105, %[rx], s40 offen lds\n\t"
        "s_waitcnt lgkmcnt(3)\n\t"
        "v_mfma_f32_16x16x32_bf16 v[84:87], a[48:51], v[68:71], v[84:87]\n\t"
        "s_add_u32 m0, s50, 12288\n\t"
        "s_cmp_ge_u32 s44, 2\n\t"
        "s_cselect_b32 s40, s61, s60\n\t"
        "s_lshl_b32 s40, s40, 7\n\t"
        "s_add_u32 s40, s40, s51\n\t"
        "buffer_load_dwordx4 v105, %[rx], s40 offen lds\n\t"
        "v_mfma_f32_16x16x32_bf16 v[88:91], a[52:55], v[68:71], v[88:91]\n\t"
        "ds_read_b128 v[68:71], v117 offset:10240\n\t"
        "s_add_u32 s40, s45, 10\n\t"
        "s_add_u32 s46, %[start], s40\n\t"
        "s_cmp_ge_u32 s46, %[nblk]\n\t"
        "s_cselect_b32 s43, %[nblk], 0\n\t"
        "s_sub_u32 s46, s46, s43\n\t"
        "s_add_u32 s46, s46, %[blk0]\n\t"
        "s_mul_i32 s46, s46, 147456\n\t"
        "s_waitcnt lgkmcnt(3)\n\t"
        "v_mfma_f32_16x16x32_bf16 v[84:87], a[56:59], v[72:75], v[84:87]\n\t"
        "v_cvt_pk_bf16_f32 v100, v92, v93\n\t"
        "v_mfma_f32_16x16x32_bf16 v[88:91], a[60:63], v[72:75], v[88:91]\n\t"
        "ds_read_b128 v[72:75], v118 offset:10240\n\t"
        "v_cvt_pk_bf16_f32 v101, v94, v95\n\t"
        "s_waitcnt lgkmcnt(3)\n\t"
        "v_mfma_f32_16x16x32_bf16 v[84:87], a[64:67], v[60:63], v[84:87]\n\t"
        "v_cvt_pk_bf16_f32 v102, v96, v97\n\t"
        "v_mfma_f32_16x16x32_bf16 v[88:91], a[68:71], v[60:63], v[88:91]\n\t"
        "ds_read_b128 v[60:63], v117 offset:12288\n\t"
        "v_cvt_pk_bf16_f32 v103, v98, v99\n\t"
        "s_waitcnt lgkmcnt(3)\n\t"
        "v_mfma_f32_16x16x32_bf16 v[84:87], a[72:75], v[64:67], v[84:87]\n\t"
        "buffer_store_dwordx4 v[100:103], v104, %[ro], s46 offen sc1\n\t"
        "v_mfma_f32_16x16x32_bf16 v[88:91], a[76:79], v[64:67], v[88:91]\n\t"
        "ds_read_b128 v[64:67], v118 offset:12288\n\t"
        "s_lshl_b32 s40, s45, 2\n\t"
        "s_add_u32 s40, s40, 2\n\t"
        "s_add_u32 s46, %[start], s40\n\t"
        "s_cmp_ge_u32 s46, %[nblk]\n\t"
        "s_cselect_b32 s43, %[nblk], 0\n\t"
        "s_sub_u32 s46, s46, s43\n\t"
        "s_add_u32 s46, s46, %[blk0]\n\t"
        "s_mul_i32 s46, s46, 147456\n\t"
        "s_waitcnt lgkmcnt(3)\n\t"
        "v_mfma_f32_16x16x32_bf16 v[84:87], a[80:83], v[68:71], v[84:87]\n\t"
        "v_cvt_pk_bf16_f32 v100, v32, v33\n\t"
        "v_mfma_f32_16x16x32_bf16 v[88:91], a[84:87], v[68:71], v[88:91]\n\t"
        "ds_read_b128 v[68:71], v117 offset:14336\n\t"
        "v_cvt_pk_bf16_f32 v101, v34, v35\n\t"
        "s_waitcnt lgkmcnt(3)\n\t"
        "v_mfma_f32_16x16x32_bf16 v[84:87], a[88:91], v[72:75], v[84:87]\n\t"
        "v_cvt_pk_bf16_f32 v102, v36, v37\n\t"
        "v_mfma_f32_16x16x32_bf16 v[88:91], a[92:95], v[72:75], v[88:91]\n\t"
        "ds_read_b128 v[72:75], v118 offset:14336\n\t"
        "v_cvt_pk_bf16_f32 v103, v38, v39\n\t"
        "s_waitcnt lgkmcnt(3)\n\t"
        "v_mfma_f32_16x16x32_bf16 v[84:87], a[96:99], v[60:63], v[84:87]\n\t"
        "buffer_store_dwordx4 v[100:103], v104, %[ro], s46 offen sc1\n\t"
        "v_mfma_f32_16x16x32_bf16 v[88:91], a[100:103], v[60:63], v[88:91]\n\t"
        "ds_read_b128 v[60:63], v117 offset:16384\n\t"
        "s_waitcnt lgkmcnt(3)\n\t"
        "v_mfma_f32_16x16x32_bf16 v[84:87], a[104:107], v[64:67], v[84:87]\n\t"
        "v_mfma_f32_16x16x32_bf16 v[88:91], a[108:111], v[64:67], v[88:91]\n\t"
        "ds_read_b128 v[64:67], v118 offset:16384\n\t"
        "s_waitcnt lgkmcnt(3)\n\t"
        "v_mfma_f32_16x16x32_bf16 v[84:87], a[112:115], v[68:71], v[84:87]\n\t"
        "v_mfma_f32_16x16x32_bf16 v[88:91], a[116:119], v[68:71], v[88:91]\n\t"
        "s_waitcnt vmcnt(8)\n\t"
        "s_waitcnt lgkmcnt(0)\n\t"
        "s_barrier\n\t"
        "ds_read_b128 v[68:71], v115 offset:0\n\t"
        "v_mfma_f32_16x16x32_bf16 v[84:87], a[120:123], v[72:75], v[84:87]\n\t"
        "v_mfma_f32_16x16x32_bf16 v[88:91], a[124:127], v[72:75], v[88:91]\n\t"
        "ds_read_b128 v[72:75], v116 offset:0\n\t"
        "v_mfma_f32_16x16x32_bf16 v[84:87], v[0:3], v[60:63], v[84:87]\n\t"
        "v_mfma_f32_16x16x32_bf16 v[88:91], v[4:7], v[60:63], v[88:91]\n\t"
        "ds_read_b128 v[60:63], v115 offset:2048\n\t"
        "v_mfma_f32_16x16x32_bf16 v[84:87], v[8:11], v[64:67], v[84:87]\n\t"
        "v_mfma_f32_16x16x32_bf16 v[88:91], v[12:15], v[64:67], v[88:91]\n\t"
        "ds_read_b128 v[64:67], v116 offset:2048\n\t"
        "s_waitcnt lgkmcnt(3)\n\t"
        "v_mfma_f32_16x16x32_bf16 v[92:95], a[0:3], v[68:71], 0\n\t"
        "v_mfma_f32_16x16x32_bf16 v[96:99], a[4:7], v[68:71], 0\n\t"
        "ds_read_b128 v[68:71], v115 offset:4096\n\t"
        "s_waitcnt lgkmcnt(3)\n\t"
        "v_mfma_f32_16x16x32_bf16 v[92:95], a[8:11], v[72:75], v[92:95]\n\t"
        "s_cmp_lg_u32 s45, 0\n\t"
        "v_mfma_f32_16x16x32_bf16 v[96:99], a[12:15], v[72:75], v[96:99]\n\t"
        "ds_read_b128 v[72:75], v116 offset:4096\n\t"
        "s_mov_b32 s41, 92160\n\t"
        "s_waitcnt lgkmcnt(3)\n\t"
        "v_mfma_f32_16x16x32_bf16 v[92:95], a[16:19], v[60:63], v[92:95]\n\t"
        "s_cselect_b32 s41, 0, s41\n\t"
        "v_mfma_f32_16x16x32_bf16 v[96:99], a[20:23], v[60:63], v[96:99]\n\t"
        "ds_read_b128 v[60:63], v115 offset:6144\n\t"
        "v_add_u32 v117, s41, v106\n\t"
        "s_waitcnt lgkmcnt(3)\n\t"
        "v_mfma_f32_16x16x32_bf16 v[92:95], a[24:27], v[64:67], v[92:95]\n\t"
        "v_add_u32 v118, s41, v107\n\t"
        "v_mfma_f32_16x16x32_bf16 v[96:99], a[28:31], v[64:67], v[96:99]\n\t"
        "ds_read_b128 v[64:67], v116 offset:6144\n\t"
        "s_add_u32 s40, s45, 18\n\t"
        "s_add_u32 s51, %[start], s40\n\t"
        "s_cmp_ge_u32 s51, %[nblk]\n\t"
        "s_cselect_b32 s43, %[nblk], 0\n\t"
        "s_sub_u32 s51, s51, s43\n\t"
        "s_add_u32 s51, s51, %[blk0]\n\t"
        "s_mul_i32 s51, s51, 18432\n\t"
        "s_cmp_gt_u32 %[nblk], s40\n\t"
        "s_cselect_b32 s51, s51, 0x7f000000\n\t"
        "s_mov_b32 s41, 18432\n\t"
        "s_cmp_lg_u32 s45, 0\n\t"
        "s_cselect_b32 s41, 36864, s41\n\t"
        "s_add_u32 s50, s41, s53\n\t"
        "s_waitcnt lgkmcnt(3)\n\t"
        "v_mfma_f32_16x16x32_bf16 v[92:95], a[32:35], v[68:71], v[92:95]\n\t"
        "s_cmp_ge_u32 s44, 2\n\t"
        "s_cbranch_scc1 L_x2s7_%=\n\t"
        "s_add_u32 m0, s50, 16384\n\t"
        "s_lshl_b32 s40, s62, 7\n\t"
        "s_add_u32 s40, s40, s51\n\t"
        "buffer_load_dwordx4 v105, %[rx], s40 offen lds\n\t"
        "L_x2s7_%=:\n\t"
        "v_mfma_f32_16x16x32_bf16 v[96:99], a[36:39], v[68:71], v[96:99]\n\t"
        "ds_read_b128 v[68:71], v115 offset:8192\n\t"
        "s_add_u32 m0, s50, 0\n\t"
        "s_cmp_ge_u32 s44, 2\n\t"
        "s_cselect_b32 s40, s55, s54\n\t"
        "s_lshl_b32 s40, s40, 7\n\t"
        "s_add_u32 s40, s40, s51\n\t"
        "buffer_load_dwordx4 v105, %[rx], s40 offen lds\n\t"
        "s_waitcnt lgkmcnt(3)\n\t"
        "v_mfma_f32_16x16x32_bf16 v[92:95], a[40:43], v[72:75], v[92:95]\n\t"
        "s_add_u32 m0, s50, 4096\n\t"
        "s_cmp_ge_u32 s44, 2\n\t"
        "s_cselect_b32 s40, s57, s56\n\t"
        "s_lshl_b32 s40, s40, 7\n\t"
        "s_add_u32 s40, s40, s51\n\t"
        "buffer_load_dwordx4 v105, %[rx], s40 offen lds\n\t"
        "v_mfma_f32_16x16x32_bf16 v[96:99], a[44:47], v[72:75], v[96:99]\n\t"
        "ds_read_b128 v[72:75], v116 offset:8192\n\t"
        "s_add_u32 m0, s50, 8192\n\t"
        "s_cmp_ge_u32 s44, 2\n\t"
        "s_cselect_b32 s40, s59, s58\n\t"
        "s_lshl_b32 s40, s40, 7\n\t"
        "s_add_u32 s40, s40, s51\n\t"
        "buffer_load_dwordx4 v105, %[rx], s40 offen lds\n\t"
        "s_waitcnt lgkmcnt(3)\n\t"
        "v_mfma_f32_16x16x32_bf16 v[92:95], a[48:51], v[60:63], v[92:95]\n\t"
        "s_add_u32 m0, s50, 12288\n\t"
        "s_cmp_ge_u32 s44, 2\n\t"
        "s_cselect_b32 s40, s61, s60\n\t"
        "s_lshl_b32 s40, s40, 7\n\t"
        "s_add_u32 s40, s40, s51\n\t"
        "buffer_load_dwordx4 v105, %[rx], s40 offen lds\n\t"
        "v_mfma_f32_16x16x32_bf16 v[96:99], a[52:55], v[60:63], v[96:99]\n\t"
        "ds_read_b128 v[60:63], v115 offset:10240\n\t"
        "s_add_u32 s40, s45, 12\n\t"
        "s_add_u32 s46, %[start], s40\n\t"
        "s_cmp_ge_u32 s46, %[nblk]\n\t"
        "s_cselect_b32 s43, %[nblk], 0\n\t"
        "s_sub_u32 s46, s46, s43\n\t"
        "s_add_u32 s46, s46, %[blk0]\n\t"
        "s_mul_i32 s46, s46, 147456\n\t"
        "s_waitcnt lgkmcnt(3)\n\t"
        "v_mfma_f32_16x16x32_bf16 v[92:95], a[56:59], v[64:67], v[92:95]\n\t"
        "v_cvt_pk_bf16_f32 v100, v84, v85\n\t"
        "v_mfma_f32_16x16x32_bf16 v[96:99], a[60:63], v[64:67], v[96:99]\n\t"
        "ds_read_b128 v[64:67], v116 offset:10240\n\t"
        "v_cvt_pk_bf16_f32 v101, v86, v87\n\t"
        "s_waitcnt lgkmcnt(3)\n\t"
        "v_mfma_f32_16x16x32_bf16 v[92:95], a[64:67], v[68:71], v[92:95]\n\t"
        "v_cvt_pk_bf16_f32 v102, v88, v89\n\t"
        "v_mfma_f32_16x16x32_bf16 v[96:99], a[68:71], v[68:71], v[96:99]\n\t"
        "ds_read_b128 v[68:71], v115 offset:12288\n\t"
        "v_cvt_pk_bf16_f32 v103, v90, v91\n\t"
        "s_waitcnt lgkmcnt(3)\n\t"
        "v_mfma_f32_16x16x32_bf16 v[92:95], a[72:75], v[72:75], v[92:95]\n\t"
        "buffer_store_dwordx4 v[100:103], v104, %[ro], s46 offen sc1\n\t"
        "v_mfma_f32_16x16x32_bf16 v[96:99], a[76:79], v[72:75], v[96:99]\n\t"
        "ds_read_b128 v[72:75], v116 offset:12288\n\t"
        "s_lshl_b32 s40, s45, 2\n\t"
        "s_add_u32 s40, s40, 3\n\t"
        "s_add_u32 s46, %[start], s40\n\t"
        "s_cmp_ge_u32 s46, %[nblk]\n\t"
        "s_cselect_b32 s43, %[nblk], 0\n\t"
        "s_sub_u32 s46, s46, s43\n\t"
        "s_add_u32 s46, s46, %[blk0]\n\t"
        "s_mul_i32 s46, s46, 147456\n\t"
        "s_waitcnt lgkmcnt(3)\n\t"
        "v_mfma_f32_16x16x32_bf16 v[92:95], a[80:83], v[60:63], v[92:95]\n\t"
        "v_cvt_pk_bf16_f32 v100, v40, v41\n\t"
        "v_mfma_f32_16x16x32_bf16 v[96:99], a[84:87], v[60:63], v[96:99]\n\t"
        "ds_read_b128 v[60:63], v115 offset:14336\n\t"
        "v_cvt_pk_bf16_f32 v101, v42, v43\n\t"
        "s_waitcnt lgkmcnt(3)\n\t"
        "v_mfma_f32_16x16x32_bf16 v[92:95], a[88:91], v[64:67], v[92:95]\n\t"
        "v_cvt_pk_bf16_f32 v102, v44, v45\n\t"
        "v_mfma_f32_16x16x32_bf16 v[96:99], a[92:95], v[64:67], v[96:99]\n\t"
        "ds_read_b128 v[64:67], v116 offset:14336\n\t"
        "v_cvt_pk_bf16_f32 v103, v46, v47\n\t"
        "s_waitcnt lgkmcnt(3)\n\t"
        "v_mfma_f32_16x16x32_bf16 v[92:95], a[96:99], v[68:71], v[92:95]\n\t"
        "buffer_store_dwordx4 v[100:103], v104, %[ro], s46 offen sc1\n\t"
        "v_mfma_f32_16x16x32_bf16 v[96:99], a[100:103], v[68:71], v[96:99]\n\t"
        "ds_read_b128 v[68:71], v115 offset:16384\n\t"
        "s_waitcnt lgkmcnt(3)\n\t"
        "v_mfma_f32_16x16x32_bf16 v[92:95], a[104:107], v[72:75], v[92:95]\n\t"
        "v_mfma_f32_16x16x32_bf16 v[96:99], a[108:111], v[72:75], v[96:99]\n\t"
        "ds_read_b128 v[72:75], v116 offset:16384\n\t"
        "s_waitcnt lgkmcnt(3)\n\t"
        "v_mfma_f32_16x16x32_bf16 v[92:95], a[112:115], v[60:63], v[92:95]\n\t"
        "v_mfma_f32_16x16x32_bf16 v[96:99], a[116:119], v[60:63], v[96:99]\n\t"
        "s_waitcnt vmcnt(8)\n\t"
        "s_waitcnt lgkmcnt(0)\n\t"
        "s_barrier\n\t"
        "ds_read_b128 v[60:63], v117 offset:0\n\t"
        "v_mfma_f32_16x16x32_bf16 v[92:95], a[120:123], v[64:67], v[92:95]\n\t"
        "v_mfma_f32_16x16x32_bf16 v[96:99], a[124:127], v[64:67], v[96:99]\n\t"
        "ds_read_b128 v[64:67], v118 offset:0\n\t"
        "v_mfma_f32_16x16x32_bf16 v[92:95], v[0:3], v[68:71], v[92:95]\n\t"
        "v_mfma_f32_16x16x32_bf16 v[96:99], v[4:7], v[68:71], v[96:99]\n\t"
        "ds_read_b128 v[68:71], v117 offset:2048\n\t"
        "v_mfma_f32_16x16x32_bf16 v[92:95], v[8:11], v[72:75], v[92:95]\n\t"
        "v_mfma_f32_16x16x32_bf16 v[96:99], v[12:15], v[72:75], v[96:99]\n\t"
        "ds_read_b128 v[72:75], v118 offset:2048\n\t"
        "s_waitcnt lgkmcnt(3)\n\t"
        "v_mfma_f32_16x16x32_bf16 v[84:87], a[0:3], v[60:63], 0\n\t"
        "v_mfma_f32_16x16x32_bf16 v[88:91], a[4:7], v[60:63], 0\n\t"
        "ds_read_b128 v[60:63], v117 offset:4096\n\t"
        "s_waitcnt lgkmcnt(3)\n\t"
        "v_mfma_f32_16x16x32_bf16 v[84:87], a[8:11], v[64:67], v[84:87]\n\t"
        "s_cmp_lg_u32 s45, 0\n\t"
        "v_mfma_f32_16x16x32_bf16 v[88:91], a[12:15], v[64:67], v[88:91]\n\t"
        "ds_read_b128 v[64:67], v118 offset:4096\n\t"
        "s_mov_b32 s41, 18432\n\t"
        "s_waitcnt lgkmcnt(3)\n\t"
        "v_mfma_f32_16x16x32_bf16 v[84:87], a[16:19], v[68:71], v[84:87]\n\t"
        "s_cselect_b32 s41, 18432, s41\n\t"
        "v_mfma_f32_16x16x32_bf16 v[88:91], a[20:23], v[68:71], v[88:91]\n\t"
        "ds_read_b128 v[68:71], v117 offset:6144\n\t"
        "v_add_u32 v115, s41, v106\n\t"
        "s_waitcnt lgkmcnt(3)\n\t"
        "v_mfma_f32_16x16x32_bf16 v[84:87], a[24:27], v[72:75], v[84:87]\n\t"
        "v_add_u32 v116, s41, v107\n\t"
        "v_mfma_f32_16x16x32_bf16 v[88:91], a[28:31], v[72:75], v[88:91]\n\t"
        "ds_read_b128 v[72:75], v118 offset:6144\n\t"
        "s_add_u32 s40, s45, 14\n\t"
        "s_add_u32 s46, %[start], s40\n\t"
        "s_cmp_ge_u32 s46, %[nblk]\n\t"
        "s_cselect_b32 s43, %[nblk], 0\n\t"
        "s_sub_u32 s46, s46, s43\n\t"
        "s_add_u32 s46, s46, %[blk0]\n\t"
        "s_mul_i32 s46, s46, 147456\n\t"
        "s_waitcnt lgkmcnt(3)\n\t"
        "v_mfma_f32_16x16x32_bf16 v[84:87], a[32:35], v[60:63], v[84:87]\n\t"
        "v_cvt_pk_bf16_f32 v100, v92, v93\n\t"
        "v_mfma_f32_16x16x32_bf16 v[88:91], a[36:39], v[60:63], v[88:91]\n\t"
        "ds_read_b128 v[60:63], v117 offset:8192\n\t"
        "v_cvt_pk_bf16_f32 v101, v94, v95\n\t"
        "s_waitcnt lgkmcnt(3)\n\t"
        "v_mfma_f32_16x16x32_bf16 v[84:87], a[40:43], v[64:67], v[84:87]\n\t"
        "v_cvt_pk_bf16_f32 v102, v96, v97\n\t"
        "v_mfma_f32_16x16x32_bf16 v[88:91], a[44:47], v[64:67], v[88:91]\n\t"
        "ds_read_b128 v[64:67], v118 offset:8192\n\t"
        "v_cvt_pk_bf16_f32 v103, v98, v99\n\t"
        "s_waitcnt lgkmcnt(3)\n\t"
        "v_mfma_f32_16x16x32_bf16 v[84:87], a[48:51], v[68:71], v[84:87]\n\t"
        "buffer_store_dwordx4 v[100:103], v104, %[ro], s46 offen sc1\n\t"
        "v_mfma_f32_16x16x32_bf16 v[88:91], a[52:55], v[68:71], v[88:91]\n\t"
        "ds_read_b128 v[68:71], v117 offset:10240\n\t"
        "s_waitcnt lgkmcnt(3)\n\t"
        "v_mfma_f32_16x16x32_bf16 v[84:87], a[56:59], v[72:75], v[84:87]\n\t"
        "v_mfma_f32_16x16x32_bf16 v[88:91], a[60:63], v[72:75], v[88:91]\n\t"
        "ds_read_b128 v[72:75], v118 offset:10240\n\t"
        "s_waitcnt lgkmcnt(3)\n\t"
        "v_mfma_f32_16x16x32_bf16 v[84:87], a[64:67], v[60:63], v[84:87]\n\t"
        "v_mfma_f32_16x16x32_bf16 v[88:91], a[68:71], v[60:63], v[88:91]\n\t"
        "ds_read_b128 v[60:63], v117 offset:12288\n\t"
        "s_waitcnt lgkmcnt(3)\n\t"
        "v_mfma_f32_16x16x32_bf16 v[84:87], a[72:75], v[64:67], v[84:87]\n\t"
        "v_mfma_f32_16x16x32_bf16 v[88:91], a[76:79], v[64:67], v[88:91]\n\t"
        "ds_read_b128 v[64:67], v118 offset:12288\n\t"
        "s_waitcnt lgkmcnt(3)\n\t"
        "v_mfma_f32_16x16x32_bf16 v[84:87], a[80:83], v[68:71], v[84:87]\n\t"
        "v_mfma_f32_16x16x32_bf16 v[88:91], a[84:87], v[68:71], v[88:91]\n\t"
        "ds_read_b128 v[68:71], v117 offset:14336\n\t"
        "s_waitcnt lgkmcnt(3)\n\t"
        "v_mfma_f32_16x16x32_bf16 v[84:87], a[88:91], v[72:75], v[84:87]\n\t"
        "v_mfma_f32_16x16x32_bf16 v[88:91], a[92:95], v[72:75], v[88:91]\n\t"
        "ds_read_b128 v[72:75], v118 offset:14336\n\t"
        "s_waitcnt lgkmcnt(3)\n\t"
        "v_mfma_f32_16x16x32_bf16 v[84:87], a[96:99], v[60:63], v[84:87]\n\t"
        "v_mfma_f32_16x16x32_bf16 v[88:91], a[100:103], v[60:63], v[88:91]\n\t"
        "ds_read_b128 v[60:63], v117 offset:16384\n\t"
        "s_waitcnt lgkmcnt(3)\n\t"
        "v_mfma_f32_16x16x32_bf16 v[84:87], a[104:107], v[64:67], v[84:87]\n\t"
        "v_mfma_f32_16x16x32_bf16 v[88:91], a[108:111], v[64:67], v[88:91]\n\t"
        "ds_read_b128 v[64:67], v118 offset:16384\n\t"
        "s_waitcnt lgkmcnt(3)\n\t"
        "v_mfma_f32_16x16x32_bf16 v[84:87], a[112:115], v[68:71], v[84:87]\n\t"
        "v_mfma_f32_16x16x32_bf16 v[88:91], a[116:119], v[68:71], v[88:91]\n\t"
        "s_waitcnt vmcnt(3)\n\t"
        "s_waitcnt lgkmcnt(0)\n\t"
        "s_barrier\n\t"
        "ds_read_b128 v[68:71], v115 offset:0\n\t"
        "v_mfma_f32_16x16x32_bf16 v[84:87], a[120:123], v[72:75], v[84:87]\n\t"
        "v_mfma_f32_16x16x32_bf16 v[88:91], a[124:127], v[72:75], v[88:91]\n\t"
        "ds_read_b128 v[72:75], v116 offset:0\n\t"
        "v_mfma_f32_16x16x32_bf16 v[84:87], v[0:3], v[60:63], v[84:87]\n\t"
        "v_mfma_f32_16x16x32_bf16 v[88:91], v[4:7], v[60:63], v[88:91]\n\t"
        "ds_read_b128 v[60:63], v115 offset:2048\n\t"
        "v_mfma_f32_16x16x32_bf16 v[84:87], v[8:11], v[64:67], v[84:87]\n\t"
        "v_mfma_f32_16x16x32_bf16 v[88:91], v[12:15], v[64:67], v[88:91]\n\t"
        "ds_read_b128 v[64:67], v116 offset:2048\n\t"
        "s_cmp_lt_u32 %[nblk], 19\n\t"
        "s_cbranch_scc1 L_skip_last_%=\n\t"
        "s_cmp_lg_u32 s45, 0\n\t"
        "s_cbranch_scc1 L_last1_%=\n\t"
        "s_waitcnt lgkmcnt(3)\n\t"
        "v_mfma_f32_16x16x32_bf16 v[92:95], a[0:3], v[68:71], 0\n\t"
        "ds_read_b128 v[68:71], v115 offset:4096\n\t"
        "s_waitcnt lgkmcnt(3)\n\t"
        "v_mfma_f32_16x16x32_bf16 v[92:95], a[8:11], v[72:75], v[92:95]\n\t"
        "ds_read_b128 v[72:75], v116 offset:4096\n\t"
        "s_waitcnt lgkmcnt(3)\n\t"
        "v_mfma_f32_16x16x32_bf16 v[92:95], a[16:19], v[60:63], v[92:95]\n\t"
        "ds_read_b128 v[60:63], v115 offset:6144\n\t"
        "s_waitcnt lgkmcnt(3)\n\t"
        "v_mfma_f32_16x16x32_bf16 v[92:95], a[24:27], v[64:67], v[92:95]\n\t"
        "ds_read_b128 v[64:67], v116 offset:6144\n\t"
        "s_waitcnt lgkmcnt(3)\n\t"
        "v_mfma_f32_16x16x32_bf16 v[92:95], a[32:35], v[68:71], v[92:95]\n\t"
        "ds_read_b128 v[68:71], v115 offset:8192\n\t"
        "s_add_u32 s40, s45, 16\n\t"
        "s_add_u32 s46, %[start], s40\n\t"
        "s_cmp_ge_u32 s46, %[nblk]\n\t"
        "s_cselect_b32 s43, %[nblk], 0\n\t"
        "s_sub_u32 s46, s46, s43\n\t"
        "s_add_u32 s46, s46, %[blk0]\n\t"
        "s_mul_i32 s46, s46, 147456\n\t"
        "s_waitcnt lgkmcnt(3)\n\t"
        "v_mfma_f32_16x16x32_bf16 v[92:95], a[40:43], v[72:75], v[92:95]\n\t"
        "ds_read_b128 v[72:75], v116 offset:8192\n\t"
        "v_cvt_pk_bf16_f32 v100, v84, v85\n\t"
        "s_waitcnt lgkmcnt(3)\n\t"
        "v_mfma_f32_16x16x32_bf16 v[92:95], a[48:51], v[60:63], v[92:95]\n\t"
        "ds_read_b128 v[60:63], v115 offset:10240\n\t"
        "v_cvt_pk_bf16_f32 v101, v86, v87\n\t"
        "s_waitcnt lgkmcnt(3)\n\t"
        "v_mfma_f32_16x16x32_bf16 v[92:95], a[56:59], v[64:67], v[92:95]\n\t"
        "ds_read_b128 v[64:67], v116 offset:10240\n\t"
        "v_cvt_pk_bf16_f32 v102, v88, v89\n\t"
        "s_waitcnt lgkmcnt(3)\n\t"
        "v_mfma_f32_16x16x32_bf16 v[92:95], a[64:67], v[68:71], v[92:95]\n\t"
        "ds_read_b128 v[68:71], v115 offset:12288\n\t"
        "v_cvt_pk_bf16_f32 v103, v90, v91\n\t"
        "s_waitcnt lgkmcnt(3)\n\t"
        "v_mfma_f32_16x16x32_bf16 v[92:95], a[72:75], v[72:75], v[92:95]\n\t"
        "ds_read_b128 v[72:75], v116 offset:12288\n\t"
        "buffer_store_dwordx4 v[100:103], v104, %[ro], s46 offen sc1\n\t"
        "s_waitcnt lgkmcnt(3)\n\t"
        "v_mfma_f32_16x16x32_bf16 v[92:95], a[80:83], v[60:63], v[92:95]\n\t"
        "ds_read_b128 v[60:63], v115 offset:14336\n\t"
        "s_waitcnt lgkmcnt(3)\n\t"
        "v_mfma_f32_16x16x32_bf16 v[92:95], a[88:91], v[64:67], v[92:95]\n\t"
        "ds_read_b128 v[64:67], v116 offset:14336\n\t"
        "s_waitcnt lgkmcnt(3)\n\t"
        "v_mfma_f32_16x16x32_bf16 v[92:95], a[96:99], v[68:71], v[92:95]\n\t"
        "ds_read_b128 v[68:71], v115 offset:16384\n\t"
        "s_waitcnt lgkmcnt(3)\n\t"
        "v_mfma_f32_16x16x32_bf16 v[92:95], a[104:107], v[72:75], v[92:95]\n\t"
        "ds_read_b128 v[72:75], v116 offset:16384\n\t"
        "s_waitcnt lgkmcnt(3)\n\t"
        "v_mfma_f32_16x16x32_bf16 v[92:95], a[112:115], v[60:63], v[92:95]\n\t"
        "s_waitcnt lgkmcnt(2)\n\t"
        "v_mfma_f32_16x16x32_bf16 v[92:95], a[120:123], v[64:67], v[92:95]\n\t"
        "s_waitcnt lgkmcnt(1)\n\t"
        "v_mfma_f32_16x16x32_bf16 v[92:95], v[0:3], v[68:71], v[92:95]\n\t"
        "s_waitcnt lgkmcnt(0)\n\t"
        "v_mfma_f32_16x16x32_bf16 v[92:95], v[8:11], v[72:75], v[92:95]\n\t"
        "s_add_u32 s46, %[start], 18\n\t"
        "s_cmp_ge_u32 s46, %[nblk]\n\t"
        "s_cselect_b32 s43, %[nblk], 0\n\t"
        "s_sub_u32 s46, s46, s43\n\t"
        "s_add_u32 s46, s46, %[blk0]\n\t"
        "s_mul_i32 s46, s46, 147456\n\t"
        "s_nop 7\n\t"
        "v_cvt_pk_bf16_f32 v100, v92, v93\n\t"
        "v_cvt_pk_bf16_f32 v101, v94, v95\n\t"
        "buffer_store_dwordx2 v[100:101], v104, %[ro], s46 offen offset:0 sc1\n\t"
        "s_branch L_cend_%=\n\t"
        "L_last1_%=:\n\t"
        "s_waitcnt lgkmcnt(3)\n\t"
        "v_mfma_f32_16x16x32_bf16 v[92:95], a[4:7], v[68:71], 0\n\t"
        "ds_read_b128 v[68:71], v115 offset:4096\n\t"
        "s_waitcnt lgkmcnt(3)\n\t"
        "v_mfma_f32_16x16x32_bf16 v[92:95], a[12:15], v[72:75], v[92:95]\n\t"
        "ds_read_b128 v[72:75], v116 offset:4096\n\t"
        "s_waitcnt lgkmcnt(3)\n\t"
        "v_mfma_f32_16x16x32_bf16 v[92:95], a[20:23], v[60:63], v[92:95]\n\t"
        "ds_read_b128 v[60:63], v115 offset:6144\n\t"
        "s_waitcnt lgkmcnt(3)\n\t"
        "v_mfma_f32_16x16x32_bf16 v[92:95], a[28:31], v[64:67], v[92:95]\n\t"
        "ds_read_b128 v[64:67], v116 offset:6144\n\t"
        "s_waitcnt lgkmcnt(3)\n\t"
        "v_mfma_f32_16x16x32_bf16 v[92:95], a[36:39], v[68:71], v[92:95]\n\t"
        "ds_read_b128 v[68:71], v115 offset:8192\n\t"
        "s_add_u32 s40, s45, 16\n\t"
        "s_add_u32 s46, %[start], s40\n\t"
        "s_cmp_ge_u32 s46, %[nblk]\n\t"
        "s_cselect_b32 s43, %[nblk], 0\n\t"
        "s_sub_u32 s46, s46, s43\n\t"
        "s_add_u32 s46, s46, %[blk0]\n\t"
        "s_mul_i32 s46, s46, 147456\n\t"
        "s_waitcnt lgkmcnt(3)\n\t"
        "v_mfma_f32_16x16x32_bf16 v[92:95], a[44:47], v[72:75], v[92:95]\n\t"
        "ds_read_b128 v[72:75], v116 offset:8192\n\t"
        "v_cvt_pk_bf16_f32 v100, v84, v85\n\t"
        "s_waitcnt lgkmcnt(3)\n\t"
        "v_mfma_f32_16x16x32_bf16 v[92:95], a[52:55], v[60:63], v[92:95]\n\t"
        "ds_read_b128 v[60:63], v115 offset:10240\n\t"
        "v_cvt_pk_bf16_f32 v101, v86, v87\n\t"
        "s_waitcnt lgkmcnt(3)\n\t"
        "v_mfma_f32_16x16x32_bf16 v[92:95], a[60:63], v[64:67], v[92:95]\n\t"
        "ds_read_b128 v[64:67], v116 offset:10240\n\t"
        "v_cvt_pk_bf16_f32 v102, v88, v89\n\t"
        "s_waitcnt lgkmcnt(3)\n\t"
        "v_mfma_f32_16x16x32_bf16 v[92:95], a[68:71], v[68:71], v[92:95]\n\t"
        "ds_read_b128 v[68:71], v115 offset:12288\n\t"
        "v_cvt_pk_bf16_f32 v103, v90, v91\n\t"
        "s_waitcnt lgkmcnt(3)\n\t"
        "v_mfma_f32_16x16x32_bf16 v[92:95], a[76:79], v[72:75], v[92:95]\n\t"
        "ds_read_b128 v[72:75], v116 offset:12288\n\t"
        "buffer_store_dwordx4 v[100:103], v104, %[ro], s46 offen sc1\n\t"
        "s_waitcnt lgkmcnt(3)\n\t"
        "v_mfma_f32_16x16x32_bf16 v[92:95], a[84:87], v[60:63], v[92:95]\n\t"
        "ds_read_b128 v[60:63], v115 offset:14336\n\t"
        "s_waitcnt lgkmcnt(3)\n\t"
        "v_mfma_f32_16x16x32_bf16 v[92:95], a[92:95], v[64:67], v[92:95]\n\t"
        "ds_read_b128 v[64:67], v116 offset:14336\n\t"
        "s_waitcnt lgkmcnt(3)\n\t"
        "v_mfma_f32_16x16x32_bf16 v[92:95], a[100:103], v[68:71], v[92:95]\n\t"
        "ds_read_b128 v[68:71], v115 offset:16384\n\t"
        "s_waitcnt lgkmcnt(3)\n\t"
        "v_mfma_f32_16x16x32_bf16 v[92:95], a[108:111], v[72:75], v[92:95]\n\t"
        "ds_read_b128 v[72:75], v116 offset:16384\n\t"
        "s_waitcnt lgkmcnt(3)\n\t"
        "v_mfma_f32_16x16x32_bf16 v[92:95], a[116:119], v[60:63], v[92:95]\n\t"
        "s_waitcnt lgkmcnt(2)\n\t"
        "v_mfma_f32_16x16x32_bf16 v[92:95], a[124:127], v[64:67], v[92:95]\n\t"
        "s_waitcnt lgkmcnt(1)\n\t"
        "v_mfma_f32_16x16x32_bf16 v[92:95], v[4:7], v[68:71], v[92:95]\n\t"
        "s_waitcnt lgkmcnt(0)\n\t"
        "v_mfma_f32_16x16x32_bf16 v[92:95], v[12:15], v[72:75], v[92:95]\n\t"
        "s_add_u32 s46, %[start], 18\n\t"
        "s_cmp_ge_u32 s46, %[nblk]\n\t"
        "s_cselect_b32 s43, %[nblk], 0\n\t"
        "s_sub_u32 s46, s46, s43\n\t"
        "s_add_u32 s46, s46, %[blk0]\n\t"
        "s_mul_i32 s46, s46, 147456\n\t"
        "s_nop 7\n\t"
        "v_cvt_pk_bf16_f32 v100, v92, v93\n\t"
        "v_cvt_pk_bf16_f32 v101, v94, v95\n\t"
        "buffer_store_dwordx2 v[100:101], v104, %[ro], s46 offen offset:8 sc1\n\t"
        "s_branch L_cend_%=\n\t"
        "L_skip_last_%=:\n\t"
        "s_nop 7\n\t"
        "s_add_u32 s40, s45, 16\n\t"
        "s_add_u32 s46, %[start], s40\n\t"
        "s_cmp_ge_u32 s46, %[nblk]\n\t"
        "s_cselect_b32 s43, %[nblk], 0\n\t"
        "s_sub_u32 s46, s46, s43\n\t"
        "s_add_u32 s46, s46, %[blk0]\n\t"
        "s_mul_i32 s46, s46, 147456\n\t"
        "v_cvt_pk_bf16_f32 v100, v84, v85\n\t"
        "v_cvt_pk_bf16_f32 v101, v86, v87\n\t"
        "v_cvt_pk_bf16_f32 v102, v88, v89\n\t"
        "v_cvt_pk_bf16_f32 v103, v90, v91\n\t"
        "buffer_store_dwordx4 v[100:103], v104, %[ro], s46 offen sc1\n\t"
        "L_cend_%=:\n\t"
        "s_waitcnt lgkmcnt(0)\n\t"
        "s_waitcnt vmcnt(0)\n\t"
        "s_branch L_end_%=\n\t"
        "L_hot_end_%=:\n\t"
        "L_slow0_%=:\n\t"
        "v_subrev_u32 v124, 143360, v121\n\t"
        "v_lshrrev_b32 v124, 1, v124\n\t"
        "s_mul_i32 s40, s54, 9216\n\t"
        "s_lshl_b32 s41, %[cgb], 1\n\t"
        "s_add_u32 s40, s40, s41\n\t"
        "buffer_load_dwordx2 v[92:93], v124, %[rs], s40 offen\n\t"
        "s_waitcnt vmcnt(0)\n\t"
        "v_lshlrev_b32 v100, 16, v92\n\t"
        "v_and_b32 v101, 0xffff0000, v92\n\t"
        "v_lshlrev_b32 v102, 16, v93\n\t"
        "v_and_b32 v103, 0xffff0000, v93\n\t"
        "buffer_load_dwordx2 v[92:93], v124, %[rz], s40 offen\n\t"
        "s_waitcnt vmcnt(0)\n\t"
        "v_lshlrev_b32 v104, 16, v92\n\t"
        "v_and_b32 v105, 0xffff0000, v92\n\t"
        "v_lshlrev_b32 v106, 16, v93\n\t"
        "v_and_b32 v107, 0xffff0000, v93\n\t"
        "s_mul_i32 s52, s56, 1179648\n\t"
        "v_cvt_f32_i32 v60, v60\n\t"
        "v_cvt_f32_i32 v61, v61\n\t"
        "v_cvt_f32_i32 v62, v62\n\t"
        "v_cvt_f32_i32 v63, v63\n\t"
        "v_cvt_f32_i32 v64, v64\n\t"
        "v_cvt_f32_i32 v65, v65\n\t"
        "v_cvt_f32_i32 v66, v66\n\t"
        "v_cvt_f32_i32 v67, v67\n\t"
        "v_cvt_pk_bf16_f32 v60, 0, v60\n\t"
        "v_cvt_pk_bf16_f32 v61, 0, v61\n\t"
        "v_cvt_pk_bf16_f32 v62, 0, v62\n\t"
        "v_cvt_pk_bf16_f32 v63, 0, v63\n\t"
        "v_cvt_pk_bf16_f32 v64, 0, v64\n\t"
        "v_cvt_pk_bf16_f32 v65, 0, v65\n\t"
        "v_cvt_pk_bf16_f32 v66, 0, v66\n\t"
        "v_cvt_pk_bf16_f32 v67, 0, v67\n\t"
        "v_sub_f32 v60, v60, v104\n\t"
        "v_sub_f32 v61, v61, v105\n\t"
        "v_sub_f32 v62, v62, v106\n\t"
        "v_sub_f32 v63, v63, v107\n\t"
        "v_sub_f32 v64, v64, v104\n\t"
        "v_sub_f32 v65, v65, v105\n\t"
        "v_sub_f32 v66, v66, v106\n\t"
        "v_sub_f32 v67, v67, v107\n\t"
        "v_cvt_pk_bf16_f32 v60, 0, v60\n\t"
        "v_cvt_pk_bf16_f32 v61, 0, v61\n\t"
        "v_cvt_pk_bf16_f32 v62, 0, v62\n\t"
        "v_cvt_pk_bf16_f32 v63, 0, v63\n\t"
        "v_cvt_pk_bf16_f32 v64, 0, v64\n\t"
        "v_cvt_pk_bf16_f32 v65, 0, v65\n\t"
        "v_cvt_pk_bf16_f32 v66, 0, v66\n\t"
        "v_cvt_pk_bf16_f32 v67, 0, v67\n\t"
        "v_mul_f32 v60, v60, v100\n\t"
        "v_mul_f32 v61, v61, v101\n\t"
        "v_mul_f32 v62, v62, v102\n\t"
        "v_mul_f32 v63, v63, v103\n\t"
        "v_mul_f32 v64, v64, v100\n\t"
        "v_mul_f32 v65, v65, v101\n\t"
        "v_mul_f32 v66, v66, v102\n\t"
        "v_mul_f32 v67, v67, v103\n\t"
        "v_cvt_pk_bf16_f32 v92, v60, v64\n\t"
        "v_cvt_pk_bf16_f32 v94, v61, v65\n\t"
        "v_cvt_pk_bf16_f32 v96, v62, v66\n\t"
        "v_cvt_pk_bf16_f32 v98, v63, v67\n\t"
        "buffer_load_dwordx4 v[60:63], v108, %[rq], s52 offen\n\t"
        "s_add_u32 s52, s52, 18432\n\t"
        "buffer_load_dwordx4 v[64:67], v108, %[rq], s52 offen\n\t"
        "v_cvt_f32_i32 v68, v68\n\t"
        "v_cvt_f32_i32 v69, v69\n\t"
        "v_cvt_f32_i32 v70, v70\n\t"
        "v_cvt_f32_i32 v71, v71\n\t"
        "v_cvt_f32_i32 v72, v72\n\t"
        "v_cvt_f32_i32 v73, v73\n\t"
        "v_cvt_f32_i32 v74, v74\n\t"
        "v_cvt_f32_i32 v75, v75\n\t"
        "v_cvt_pk_bf16_f32 v68, 0, v68\n\t"
        "v_cvt_pk_bf16_f32 v69, 0, v69\n\t"
        "v_cvt_pk_bf16_f32 v70, 0, v70\n\t"
        "v_cvt_pk_bf16_f32 v71, 0, v71\n\t"
        "v_cvt_pk_bf16_f32 v72, 0, v72\n\t"
        "v_cvt_pk_bf16_f32 v73, 0, v73\n\t"
        "v_cvt_pk_bf16_f32 v74, 0, v74\n\t"
        "v_cvt_pk_bf16_f32 v75, 0, v75\n\t"
        "v_sub_f32 v68, v68, v104\n\t"
        "v_sub_f32 v69, v69, v105\n\t"
        "v_sub_f32 v70, v70, v106\n\t"
        "v_sub_f32 v71, v71, v107\n\t"
        "v_sub_f32 v72, v72, v104\n\t"
        "v_sub_f32 v73, v73, v105\n\t"
        "v_sub_f32 v74, v74, v106\n\t"
        "v_sub_f32 v75, v75, v107\n\t"
        "v_cvt_pk_bf16_f32 v68, 0, v68\n\t"
        "v_cvt_pk_bf16_f32 v69, 0, v69\n\t"
        "v_cvt_pk_bf16_f32 v70, 0, v70\n\t"
        "v_cvt_pk_bf16_f32 v71, 0, v71\n\t"
        "v_cvt_pk_bf16_f32 v72, 0, v72\n\t"
        "v_cvt_pk_bf16_f32 v73, 0, v73\n\t"
        "v_cvt_pk_bf16_f32 v74, 0, v74\n\t"
        "v_cvt_pk_bf16_f32 v75, 0, v75\n\t"
        "v_mul_f32 v68, v68, v100\n\t"
        "v_mul_f32 v69, v69, v101\n\t"
        "v_mul_f32 v70, v70, v102\n\t"
        "v_mul_f32 v71, v71, v103\n\t"
        "v_mul_f32 v72, v72, v100\n\t"
        "v_mul_f32 v73, v73, v101\n\t"
        "v_mul_f32 v74, v74, v102\n\t"
        "v_mul_f32 v75, v75, v103\n\t"
        "v_cvt_pk_bf16_f32 v93, v68, v72\n\t"
        "ds_write_b64 v111, v[92:93] offset:0\n\t"
        "v_cvt_pk_bf16_f32 v95, v69, v73\n\t"
        "ds_write_b64 v112, v[94:95] offset:0\n\t"
        "v_cvt_pk_bf16_f32 v97, v70, v74\n\t"
        "ds_write_b64 v113, v[96:97] offset:0\n\t"
        "v_cvt_pk_bf16_f32 v99, v71, v75\n\t"
        "ds_write_b64 v114, v[98:99] offset:0\n\t"
        "s_add_u32 s52, s52, 18432\n\t"
        "buffer_load_dwordx4 v[68:71], v108, %[rq], s52 offen\n\t"
        "s_add_u32 s52, s52, 18432\n\t"
        "buffer_load_dwordx4 v[72:75], v108, %[rq], s52 offen\n\t"
        "s_lshl_b32 s40, s55, 9\n\t"
        "v_add_u32 v124, s40, v121\n\t"
        "ds_read_b128 v[100:103], v124\n\t"
        "ds_read_b128 v[104:107], v124 offset:4608\n\t"
        "s_add_u32 m0, s47, 36864\n\t"
        "s_lshl_b32 s40, s56, 7\n\t"
        "buffer_load_dwordx4 v109, %[rx], s40 offen lds\n\t"
        "s_add_u32 m0, s47, 45056\n\t"
        "s_lshl_b32 s40, s56, 7\n\t"
        "buffer_load_dwordx4 v110, %[rx], s40 offen lds\n\t"
        "s_waitcnt lgkmcnt(2)\n\t"
        "s_waitcnt vmcnt(12)\n\t"
        "s_waitcnt lgkmcnt(0)\n\t"
        "s_branch L_join0_%=\n\t"
        "L_slow1_%=:\n\t"
        "v_subrev_u32 v124, 143360, v121\n\t"
        "v_lshrrev_b32 v124, 1, v124\n\t"
        "s_mul_i32 s40, s55, 9216\n\t"
        "s_lshl_b32 s41, %[cgb], 1\n\t"
        "s_add_u32 s40, s40, s41\n\t"
        "buffer_load_dwordx2 v[92:93], v124, %[rs], s40 offen\n\t"
        "s_waitcnt vmcnt(0)\n\t"
        "v_lshlrev_b32 v100, 16, v92\n\t"
        "v_and_b32 v101, 0xffff0000, v92\n\t"
        "v_lshlrev_b32 v102, 16, v93\n\t"
        "v_and_b32 v103, 0xffff0000, v93\n\t"
        "buffer_load_dwordx2 v[92:93], v124, %[rz], s40 offen\n\t"
        "s_waitcnt vmcnt(0)\n\t"
        "v_lshlrev_b32 v104, 16, v92\n\t"
        "v_and_b32 v105, 0xffff0000, v92\n\t"
        "v_lshlrev_b32 v106, 16, v93\n\t"
        "v_and_b32 v107, 0xffff0000, v93\n\t"
        "s_mul_i32 s52, s57, 1179648\n\t"
        "v_cvt_f32_i32 v76, v76\n\t"
        "v_cvt_f32_i32 v77, v77\n\t"
        "v_cvt_f32_i32 v78, v78\n\t"
        "v_cvt_f32_i32 v79, v79\n\t"
        "v_cvt_f32_i32 v80, v80\n\t"
        "v_cvt_f32_i32 v81, v81\n\t"
        "v_cvt_f32_i32 v82, v82\n\t"
        "v_cvt_f32_i32 v83, v83\n\t"
        "v_cvt_pk_bf16_f32 v76, 0, v76\n\t"
        "v_cvt_pk_bf16_f32 v77, 0, v77\n\t"
        "v_cvt_pk_bf16_f32 v78, 0, v78\n\t"
        "v_cvt_pk_bf16_f32 v79, 0, v79\n\t"
        "v_cvt_pk_bf16_f32 v80, 0, v80\n\t"
        "v_cvt_pk_bf16_f32 v81, 0, v81\n\t"
        "v_cvt_pk_bf16_f32 v82, 0, v82\n\t"
        "v_cvt_pk_bf16_f32 v83, 0, v83\n\t"
        "v_sub_f32 v76, v76, v104\n\t"
        "v_sub_f32 v77, v77, v105\n\t"
        "v_sub_f32 v78, v78, v106\n\t"
        "v_sub_f32 v79, v79, v107\n\t"
        "v_sub_f32 v80, v80, v104\n\t"
        "v_sub_f32 v81, v81, v105\n\t"
        "v_sub_f32 v82, v82, v106\n\t"
        "v_sub_f32 v83, v83, v107\n\t"
        "v_cvt_pk_bf16_f32 v76, 0, v76\n\t"
        "v_cvt_pk_bf16_f32 v77, 0, v77\n\t"
        "v_cvt_pk_bf16_f32 v78, 0, v78\n\t"
        "v_cvt_pk_bf16_f32 v79, 0, v79\n\t"
        "v_cvt_pk_bf16_f32 v80, 0, v80\n\t"
        "v_cvt_pk_bf16_f32 v81, 0, v81\n\t"
        "v_cvt_pk_bf16_f32 v82, 0, v82\n\t"
        "v_cvt_pk_bf16_f32 v83, 0, v83\n\t"
        "v_mul_f32 v76, v76, v100\n\t"
        "v_mul_f32 v77, v77, v101\n\t"
        "v_mul_f32 v78, v78, v102\n\t"
        "v_mul_f32 v79, v79, v103\n\t"
        "v_mul_f32 v80, v80, v100\n\t"
        "v_mul_f32 v81, v81, v101\n\t"
        "v_mul_f32 v82, v82, v102\n\t"
        "v_mul_f32 v83, v83, v103\n\t"
        "v_cvt_pk_bf16_f32 v92, v76, v80\n\t"
        "v_cvt_pk_bf16_f32 v94, v77, v81\n\t"
        "v_cvt_pk_bf16_f32 v96, v78, v82\n\t"
        "v_cvt_pk_bf16_f32 v98, v79, v83\n\t"
        "buffer_load_dwordx4 v[76:79], v108, %[rq], s52 offen\n\t"
        "s_add_u32 s52, s52, 18432\n\t"
        "buffer_load_dwordx4 v[80:83], v108, %[rq], s52 offen\n\t"
        "v_cvt_f32_i32 v84, v84\n\t"
        "v_cvt_f32_i32 v85, v85\n\t"
        "v_cvt_f32_i32 v86, v86\n\t"
        "v_cvt_f32_i32 v87, v87\n\t"
        "v_cvt_f32_i32 v88, v88\n\t"
        "v_cvt_f32_i32 v89, v89\n\t"
        "v_cvt_f32_i32 v90, v90\n\t"
        "v_cvt_f32_i32 v91, v91\n\t"
        "v_cvt_pk_bf16_f32 v84, 0, v84\n\t"
        "v_cvt_pk_bf16_f32 v85, 0, v85\n\t"
        "v_cvt_pk_bf16_f32 v86, 0, v86\n\t"
        "v_cvt_pk_bf16_f32 v87, 0, v87\n\t"
        "v_cvt_pk_bf16_f32 v88, 0, v88\n\t"
        "v_cvt_pk_bf16_f32 v89, 0, v89\n\t"
        "v_cvt_pk_bf16_f32 v90, 0, v90\n\t"
        "v_cvt_pk_bf16_f32 v91, 0, v91\n\t"
        "v_sub_f32 v84, v84, v104\n\t"
        "v_sub_f32 v85, v85, v105\n\t"
        "v_sub_f32 v86, v86, v106\n\t"
        "v_sub_f32 v87, v87, v107\n\t"
        "v_sub_f32 v88, v88, v104\n\t"
        "v_sub_f32 v89, v89, v105\n\t"
        "v_sub_f32 v90, v90, v106\n\t"
        "v_sub_f32 v91, v91, v107\n\t"
        "v_cvt_pk_bf16_f32 v84, 0, v84\n\t"
        "v_cvt_pk_bf16_f32 v85, 0, v85\n\t"
        "v_cvt_pk_bf16_f32 v86, 0, v86\n\t"
        "v_cvt_pk_bf16_f32 v87, 0, v87\n\t"
        "v_cvt_pk_bf16_f32 v88, 0, v88\n\t"
        "v_cvt_pk_bf16_f32 v89, 0, v89\n\t"
        "v_cvt_pk_bf16_f32 v90, 0, v90\n\t"
        "v_cvt_pk_bf16_f32 v91, 0, v91\n\t"
        "v_mul_f32 v84, v84, v100\n\t"
        "v_mul_f32 v85, v85, v101\n\t"
        "v_mul_f32 v86, v86, v102\n\t"
        "v_mul_f32 v87, v87, v103\n\t"
        "v_mul_f32 v88, v88, v100\n\t"
        "v_mul_f32 v89, v89, v101\n\t"
        "v_mul_f32 v90, v90, v102\n\t"
        "v_mul_f32 v91, v91, v103\n\t"
        "v_cvt_pk_bf16_f32 v93, v84, v88\n\t"
        "ds_write_b64 v111, v[92:93] offset:16384\n\t"
        "v_cvt_pk_bf16_f32 v95, v85, v89\n\t"
        "ds_write_b64 v112, v[94:95] offset:16384\n\t"
        "v_cvt_pk_bf16_f32 v97, v86, v90\n\t"
        "ds_write_b64 v113, v[96:97] offset:16384\n\t"
        "v_cvt_pk_bf16_f32 v99, v87, v91\n\t"
        "ds_write_b64 v114, v[98:99] offset:16384\n\t"
        "s_add_u32 s52, s52, 18432\n\t"
        "buffer_load_dwordx4 v[84:87], v108, %[rq], s52 offen\n\t"
        "s_add_u32 s52, s52, 18432\n\t"
        "buffer_load_dwordx4 v[88:91], v108, %[rq], s52 offen\n\t"
        "s_lshl_b32 s40, s56, 9\n\t"
        "v_add_u32 v124, s40, v121\n\t"
        "ds_read_b128 v[100:103], v124\n\t"
        "ds_read_b128 v[104:107], v124 offset:4608\n\t"
        "s_add_u32 m0, s47, 55296\n\t"
        "s_lshl_b32 s40, s57, 7\n\t"
        "buffer_load_dwordx4 v109, %[rx], s40 offen lds\n\t"
        "s_add_u32 m0, s47, 63488\n\t"
        "s_lshl_b32 s40, s57, 7\n\t"
        "buffer_load_dwordx4 v110, %[rx], s40 offen lds\n\t"
        "v_mfma_f32_16x16x32_bf16 v[20:23], a[4:7], v[48:51], 0\n\t"
        "ds_read_b128 v[48:51], v115 offset:6144\n\t"
        "s_waitcnt lgkmcnt(10)\n\t"
        "v_mfma_f32_16x16x32_bf16 v[24:27], a[0:3], v[52:55], 0\n\t"
        "v_mfma_f32_16x16x32_bf16 v[28:31], a[4:7], v[52:55], 0\n\t"
        "ds_read_b128 v[52:55], v116 offset:0\n\t"
        "s_waitcnt lgkmcnt(8)\n\t"
        "v_mfma_f32_16x16x32_bf16 v[32:35], a[0:3], v[56:59], 0\n\t"
        "v_mfma_f32_16x16x32_bf16 v[36:39], a[4:7], v[56:59], 0\n\t"
        "ds_read_b128 v[56:59], v116 offset:2048\n\t"
        "s_waitcnt lgkmcnt(2)\n\t"
        "v_mfma_f32_16x16x32_bf16 v[40:43], a[0:3], v[48:51], 0\n\t"
        "v_mfma_f32_16x16x32_bf16 v[44:47], a[4:7], v[48:51], 0\n\t"
        "ds_read_b128 v[48:51], v116 offset:4096\n\t"
        "s_waitcnt lgkmcnt(2)\n\t"
        "v_mfma_f32_16x16x32_bf16 v[16:19], a[8:11], v[52:55], v[16:19]\n\t"
        "v_mfma_f32_16x16x32_bf16 v[20:23], a[12:15], v[52:55], v[20:23]\n\t"
        "ds_read_b128 v[52:55], v116 offset:6144\n\t"
        "s_waitcnt vmcnt(12)\n\t"
        "s_waitcnt lgkmcnt(0)\n\t"
        "s_branch L_join1_%=\n\t"
        "L_slow2_%=:\n\t"
        "v_subrev_u32 v124, 143360, v121\n\t"
        "v_lshrrev_b32 v124, 1, v124\n\t"
        "s_mul_i32 s40, s56, 9216\n\t"
        "s_lshl_b32 s41, %[cgb], 1\n\t"
        "s_add_u32 s40, s40, s41\n\t"
        "buffer_load_dwordx2 v[92:93], v124, %[rs], s40 offen\n\t"
        "s_waitcnt vmcnt(0)\n\t"
        "v_lshlrev_b32 v100, 16, v92\n\t"
        "v_and_b32 v101, 0xffff0000, v92\n\t"
        "v_lshlrev_b32 v102, 16, v93\n\t"
        "v_and_b32 v103, 0xffff0000, v93\n\t"
        "buffer_load_dwordx2 v[92:93], v124, %[rz], s40 offen\n\t"
        "s_waitcnt vmcnt(0)\n\t"
        "v_lshlrev_b32 v104, 16, v92\n\t"
        "v_and_b32 v105, 0xffff0000, v92\n\t"
        "v_lshlrev_b32 v106, 16, v93\n\t"
        "v_and_b32 v107, 0xffff0000, v93\n\t"
        "s_mul_i32 s52, s58, 1179648\n\t"
        "v_cvt_f32_i32 v60, v60\n\t"
        "v_cvt_f32_i32 v61, v61\n\t"
        "v_cvt_f32_i32 v62, v62\n\t"
        "v_cvt_f32_i32 v63, v63\n\t"
        "v_cvt_f32_i32 v64, v64\n\t"
        "v_cvt_f32_i32 v65, v65\n\t"
        "v_cvt_f32_i32 v66, v66\n\t"
        "v_cvt_f32_i32 v67, v67\n\t"
        "v_cvt_pk_bf16_f32 v60, 0, v60\n\t"
        "v_cvt_pk_bf16_f32 v61, 0, v61\n\t"
        "v_cvt_pk_bf16_f32 v62, 0, v62\n\t"
        "v_cvt_pk_bf16_f32 v63, 0, v63\n\t"
        "v_cvt_pk_bf16_f32 v64, 0, v64\n\t"
        "v_cvt_pk_bf16_f32 v65, 0, v65\n\t"
        "v_cvt_pk_bf16_f32 v66, 0, v66\n\t"
        "v_cvt_pk_bf16_f32 v67, 0, v67\n\t"
        "v_sub_f32 v60, v60, v104\n\t"
        "v_sub_f32 v61, v61, v105\n\t"
        "v_sub_f32 v62, v62, v106\n\t"
        "v_sub_f32 v63, v63, v107\n\t"
        "v_sub_f32 v64, v64, v104\n\t"
        "v_sub_f32 v65, v65, v105\n\t"
        "v_sub_f32 v66, v66, v106\n\t"
        "v_sub_f32 v67, v67, v107\n\t"
        "v_cvt_pk_bf16_f32 v60, 0, v60\n\t"
        "v_cvt_pk_bf16_f32 v61, 0, v61\n\t"
        "v_cvt_pk_bf16_f32 v62, 0, v62\n\t"
        "v_cvt_pk_bf16_f32 v63, 0, v63\n\t"
        "v_cvt_pk_bf16_f32 v64, 0, v64\n\t"
        "v_cvt_pk_bf16_f32 v65, 0, v65\n\t"
        "v_cvt_pk_bf16_f32 v66, 0, v66\n\t"
        "v_cvt_pk_bf16_f32 v67, 0, v67\n\t"
        "v_mul_f32 v60, v60, v100\n\t"
        "v_mul_f32 v61, v61, v101\n\t"
        "v_mul_f32 v62, v62, v102\n\t"
        "v_mul_f32 v63, v63, v103\n\t"
        "v_mul_f32 v64, v64, v100\n\t"
        "v_mul_f32 v65, v65, v101\n\t"
        "v_mul_f32 v66, v66, v102\n\t"
        "v_mul_f32 v67, v67, v103\n\t"
        "v_cvt_pk_bf16_f32 v92, v60, v64\n\t"
        "v_cvt_pk_bf16_f32 v94, v61, v65\n\t"
        "v_cvt_pk_bf16_f32 v96, v62, v66\n\t"
        "v_cvt_pk_bf16_f32 v98, v63, v67\n\t"
        "buffer_load_dwordx4 v[60:63], v108, %[rq], s52 offen\n\t"
        "s_add_u32 s52, s52, 18432\n\t"
        "buffer_load_dwordx4 v[64:67], v108, %[rq], s52 offen\n\t"
        "v_cvt_f32_i32 v68, v68\n\t"
        "v_cvt_f32_i32 v69, v69\n\t"
        "v_cvt_f32_i32 v70, v70\n\t"
        "v_cvt_f32_i32 v71, v71\n\t"
        "v_cvt_f32_i32 v72, v72\n\t"
        "v_cvt_f32_i32 v73, v73\n\t"
        "v_cvt_f32_i32 v74, v74\n\t"
        "v_cvt_f32_i32 v75, v75\n\t"
        "v_cvt_pk_bf16_f32 v68, 0, v68\n\t"
        "v_cvt_pk_bf16_f32 v69, 0, v69\n\t"
        "v_cvt_pk_bf16_f32 v70, 0, v70\n\t"
        "v_cvt_pk_bf16_f32 v71, 0, v71\n\t"
        "v_cvt_pk_bf16_f32 v72, 0, v72\n\t"
        "v_cvt_pk_bf16_f32 v73, 0, v73\n\t"
        "v_cvt_pk_bf16_f32 v74, 0, v74\n\t"
        "v_cvt_pk_bf16_f32 v75, 0, v75\n\t"
        "v_sub_f32 v68, v68, v104\n\t"
        "v_sub_f32 v69, v69, v105\n\t"
        "v_sub_f32 v70, v70, v106\n\t"
        "v_sub_f32 v71, v71, v107\n\t"
        "v_sub_f32 v72, v72, v104\n\t"
        "v_sub_f32 v73, v73, v105\n\t"
        "v_sub_f32 v74, v74, v106\n\t"
        "v_sub_f32 v75, v75, v107\n\t"
        "v_cvt_pk_bf16_f32 v68, 0, v68\n\t"
        "v_cvt_pk_bf16_f32 v69, 0, v69\n\t"
        "v_cvt_pk_bf16_f32 v70, 0, v70\n\t"
        "v_cvt_pk_bf16_f32 v71, 0, v71\n\t"
        "v_cvt_pk_bf16_f32 v72, 0, v72\n\t"
        "v_cvt_pk_bf16_f32 v73, 0, v73\n\t"
        "v_cvt_pk_bf16_f32 v74, 0, v74\n\t"
        "v_cvt_pk_bf16_f32 v75, 0, v75\n\t"
        "v_mul_f32 v68, v68, v100\n\t"
        "v_mul_f32 v69, v69, v101\n\t"
        "v_mul_f32 v70, v70, v102\n\t"
        "v_mul_f32 v71, v71, v103\n\t"
        "v_mul_f32 v72, v72, v100\n\t"
        "v_mul_f32 v73, v73, v101\n\t"
        "v_mul_f32 v74, v74, v102\n\t"
        "v_mul_f32 v75, v75, v103\n\t"
        "v_cvt_pk_bf16_f32 v93, v68, v72\n\t"
        "ds_write_b64 v111, v[92:93] offset:0\n\t"
        "v_cvt_pk_bf16_f32 v95, v69, v73\n\t"
        "ds_write_b64 v112, v[94:95] offset:0\n\t"
        "v_cvt_pk_bf16_f32 v97, v70, v74\n\t"
        "ds_write_b64 v113, v[96:97] offset:0\n\t"
        "v_cvt_pk_bf16_f32 v99, v71, v75\n\t"
        "ds_write_b64 v114, v[98:99] offset:0\n\t"
        "s_add_u32 s52, s52, 18432\n\t"
        "buffer_load_dwordx4 v[68:71], v108, %[rq], s52 offen\n\t"
        "s_add_u32 s52, s52, 18432\n\t"
        "buffer_load_dwordx4 v[72:75], v108, %[rq], s52 offen\n\t"
        "s_lshl_b32 s40, s57, 9\n\t"
        "v_add_u32 v124, s40, v121\n\t"
        "ds_read_b128 v[100:103], v124\n\t"
        "ds_read_b128 v[104:107], v124 offset:4608\n\t"
        "s_add_u32 m0, s47, 73728\n\t"
        "s_lshl_b32 s40, s58, 7\n\t"
        "buffer_load_dwordx4 v109, %[rx], s40 offen lds\n\t"
        "s_add_u32 m0, s47, 81920\n\t"
        "s_lshl_b32 s40, s58, 7\n\t"
        "buffer_load_dwordx4 v110, %[rx], s40 offen lds\n\t"
        "ds_read_b128 v[48:51], v115 offset:20480\n\t"
        "ds_read_b128 a[24:27], v119 offset:24576\n\t"
        "ds_read_b128 a[28:31], v120 offset:24576\n\t"
        "s_waitcnt lgkmcnt(12)\n\t"
        "v_mfma_f32_16x16x32_bf16 v[40:43], a[8:11], v[52:55], v[40:43]\n\t"
        "v_mfma_f32_16x16x32_bf16 v[44:47], a[12:15], v[52:55], v[44:47]\n\t"
        "ds_read_b128 v[52:55], v115 offset:22528\n\t"
        "s_waitcnt lgkmcnt(11)\n\t"
        "s_waitcnt lgkmcnt(10)\n\t"
        "v_mfma_f32_16x16x32_bf16 v[16:19], a[16:19], v[56:59], v[16:19]\n\t"
        "v_mfma_f32_16x16x32_bf16 v[20:23], a[20:23], v[56:59], v[20:23]\n\t"
        "ds_read_b128 v[56:59], v115 offset:24576\n\t"
        "s_waitcnt lgkmcnt(4)\n\t"
        "v_mfma_f32_16x16x32_bf16 v[24:27], a[16:19], v[48:51], v[24:27]\n\t"
        "v_mfma_f32_16x16x32_bf16 v[28:31], a[20:23], v[48:51], v[28:31]\n\t"
        "ds_read_b128 v[48:51], v116 offset:18432\n\t"
        "s_waitcnt lgkmcnt(2)\n\t"
        "v_mfma_f32_16x16x32_bf16 v[32:35], a[16:19], v[52:55], v[32:35]\n\t"
        "v_mfma_f32_16x16x32_bf16 v[36:39], a[20:23], v[52:55], v[36:39]\n\t"
        "ds_read_b128 v[52:55], v116 offset:20480\n\t"
        "s_waitcnt lgkmcnt(2)\n\t"
        "v_mfma_f32_16x16x32_bf16 v[40:43], a[16:19], v[56:59], v[40:43]\n\t"
        "v_mfma_f32_16x16x32_bf16 v[44:47], a[20:23], v[56:59], v[44:47]\n\t"
        "ds_read_b128 v[56:59], v116 offset:22528\n\t"
        "s_waitcnt lgkmcnt(2)\n\t"
        "v_mfma_f32_16x16x32_bf16 v[16:19], a[24:27], v[48:51], v[16:19]\n\t"
        "v_mfma_f32_16x16x32_bf16 v[20:23], a[28:31], v[48:51], v[20:23]\n\t"
        "ds_read_b128 v[48:51], v116 offset:24576\n\t"
        "s_waitcnt vmcnt(12)\n\t"
        "s_waitcnt lgkmcnt(0)\n\t"
        "s_branch L_join2_%=\n\t"
        "L_slow3_%=:\n\t"
        "v_subrev_u32 v124, 143360, v121\n\t"
        "v_lshrrev_b32 v124, 1, v124\n\t"
        "s_mul_i32 s40, s57, 9216\n\t"
        "s_lshl_b32 s41, %[cgb], 1\n\t"
        "s_add_u32 s40, s40, s41\n\t"
        "buffer_load_dwordx2 v[92:93], v124, %[rs], s40 offen\n\t"
        "s_waitcnt vmcnt(0)\n\t"
        "v_lshlrev_b32 v100, 16, v92\n\t"
        "v_and_b32 v101, 0xffff0000, v92\n\t"
        "v_lshlrev_b32 v102, 16, v93\n\t"
        "v_and_b32 v103, 0xffff0000, v93\n\t"
        "buffer_load_dwordx2 v[92:93], v124, %[rz], s40 offen\n\t"
        "s_waitcnt vmcnt(0)\n\t"
        "v_lshlrev_b32 v104, 16, v92\n\t"
        "v_and_b32 v105, 0xffff0000, v92\n\t"
        "v_lshlrev_b32 v106, 16, v93\n\t"
        "v_and_b32 v107, 0xffff0000, v93\n\t"
        "s_mul_i32 s52, s59, 1179648\n\t"
        "v_cvt_f32_i32 v76, v76\n\t"
        "v_cvt_f32_i32 v77, v77\n\t"
        "v_cvt_f32_i32 v78, v78\n\t"
        "v_cvt_f32_i32 v79, v79\n\t"
        "v_cvt_f32_i32 v80, v80\n\t"
        "v_cvt_f32_i32 v81, v81\n\t"
        "v_cvt_f32_i32 v82, v82\n\t"
        "v_cvt_f32_i32 v83, v83\n\t"
        "v_cvt_pk_bf16_f32 v76, 0, v76\n\t"
        "v_cvt_pk_bf16_f32 v77, 0, v77\n\t"
        "v_cvt_pk_bf16_f32 v78, 0, v78\n\t"
        "v_cvt_pk_bf16_f32 v79, 0, v79\n\t"
        "v_cvt_pk_bf16_f32 v80, 0, v80\n\t"
        "v_cvt_pk_bf16_f32 v81, 0, v81\n\t"
        "v_cvt_pk_bf16_f32 v82, 0, v82\n\t"
        "v_cvt_pk_bf16_f32 v83, 0, v83\n\t"
        "v_sub_f32 v76, v76, v104\n\t"
        "v_sub_f32 v77, v77, v105\n\t"
        "v_sub_f32 v78, v78, v106\n\t"
        "v_sub_f32 v79, v79, v107\n\t"
        "v_sub_f32 v80, v80, v104\n\t"
        "v_sub_f32 v81, v81, v105\n\t"
        "v_sub_f32 v82, v82, v106\n\t"
        "v_sub_f32 v83, v83, v107\n\t"
        "v_cvt_pk_bf16_f32 v76, 0, v76\n\t"
        "v_cvt_pk_bf16_f32 v77, 0, v77\n\t"
        "v_cvt_pk_bf16_f32 v78, 0, v78\n\t"
        "v_cvt_pk_bf16_f32 v79, 0, v79\n\t"
        "v_cvt_pk_bf16_f32 v80, 0, v80\n\t"
        "v_cvt_pk_bf16_f32 v81, 0, v81\n\t"
        "v_cvt_pk_bf16_f32 v82, 0, v82\n\t"
        "v_cvt_pk_bf16_f32 v83, 0, v83\n\t"
        "v_mul_f32 v76, v76, v100\n\t"
        "v_mul_f32 v77, v77, v101\n\t"
        "v_mul_f32 v78, v78, v102\n\t"
        "v_mul_f32 v79, v79, v103\n\t"
        "v_mul_f32 v80, v80, v100\n\t"
        "v_mul_f32 v81, v81, v101\n\t"
        "v_mul_f32 v82, v82, v102\n\t"
        "v_mul_f32 v83, v83, v103\n\t"
        "v_cvt_pk_bf16_f32 v92, v76, v80\n\t"
        "v_cvt_pk_bf16_f32 v94, v77, v81\n\t"
        "v_cvt_pk_bf16_f32 v96, v78, v82\n\t"
        "v_cvt_pk_bf16_f32 v98, v79, v83\n\t"
        "buffer_load_dwordx4 v[76:79], v108, %[rq], s52 offen\n\t"
        "s_add_u32 s52, s52, 18432\n\t"
        "buffer_load_dwordx4 v[80:83], v108, %[rq], s52 offen\n\t"
        "v_cvt_f32_i32 v84, v84\n\t"
        "v_cvt_f32_i32 v85, v85\n\t"
        "v_cvt_f32_i32 v86, v86\n\t"
        "v_cvt_f32_i32 v87, v87\n\t"
        "v_cvt_f32_i32 v88, v88\n\t"
        "v_cvt_f32_i32 v89, v89\n\t"
        "v_cvt_f32_i32 v90, v90\n\t"
        "v_cvt_f32_i32 v91, v91\n\t"
        "v_cvt_pk_bf16_f32 v84, 0, v84\n\t"
        "v_cvt_pk_bf16_f32 v85, 0, v85\n\t"
        "v_cvt_pk_bf16_f32 v86, 0, v86\n\t"
        "v_cvt_pk_bf16_f32 v87, 0, v87\n\t"
        "v_cvt_pk_bf16_f32 v88, 0, v88\n\t"
        "v_cvt_pk_bf16_f32 v89, 0, v89\n\t"
        "v_cvt_pk_bf16_f32 v90, 0, v90\n\t"
        "v_cvt_pk_bf16_f32 v91, 0, v91\n\t"
        "v_sub_f32 v84, v84, v104\n\t"
        "v_sub_f32 v85, v85, v105\n\t"
        "v_sub_f32 v86, v86, v106\n\t"
        "v_sub_f32 v87, v87, v107\n\t"
        "v_sub_f32 v88, v88, v104\n\t"
        "v_sub_f32 v89, v89, v105\n\t"
        "v_sub_f32 v90, v90, v106\n\t"
        "v_sub_f32 v91, v91, v107\n\t"
        "v_cvt_pk_bf16_f32 v84, 0, v84\n\t"
        "v_cvt_pk_bf16_f32 v85, 0, v85\n\t"
        "v_cvt_pk_bf16_f32 v86, 0, v86\n\t"
        "v_cvt_pk_bf16_f32 v87, 0, v87\n\t"
        "v_cvt_pk_bf16_f32 v88, 0, v88\n\t"
        "v_cvt_pk_bf16_f32 v89, 0, v89\n\t"
        "v_cvt_pk_bf16_f32 v90, 0, v90\n\t"
        "v_cvt_pk_bf16_f32 v91, 0, v91\n\t"
        "v_mul_f32 v84, v84, v100\n\t"
        "v_mul_f32 v85, v85, v101\n\t"
        "v_mul_f32 v86, v86, v102\n\t"
        "v_mul_f32 v87, v87, v103\n\t"
        "v_mul_f32 v88, v88, v100\n\t"
        "v_mul_f32 v89, v89, v101\n\t"
        "v_mul_f32 v90, v90, v102\n\t"
        "v_mul_f32 v91, v91, v103\n\t"
        "v_cvt_pk_bf16_f32 v93, v84, v88\n\t"
        "ds_write_b64 v111, v[92:93] offset:16384\n\t"
        "v_cvt_pk_bf16_f32 v95, v85, v89\n\t"
        "ds_write_b64 v112, v[94:95] offset:16384\n\t"
        "v_cvt_pk_bf16_f32 v97, v86, v90\n\t"
        "ds_write_b64 v113, v[96:97] offset:16384\n\t"
        "v_cvt_pk_bf16_f32 v99, v87, v91\n\t"
        "ds_write_b64 v114, v[98:99] offset:16384\n\t"
        "s_add_u32 s52, s52, 18432\n\t"
        "buffer_load_dwordx4 v[84:87], v108, %[rq], s52 offen\n\t"
        "s_add_u32 s52, s52, 18432\n\t"
        "buffer_load_dwordx4 v[88:91], v108, %[rq], s52 offen\n\t"
        "s_lshl_b32 s40, s58, 9\n\t"
        "v_add_u32 v124, s40, v121\n\t"
        "ds_read_b128 v[100:103], v124\n\t"
        "ds_read_b128 v[104:107], v124 offset:4608\n\t"
        "s_add_u32 m0, s47, 92160\n\t"
        "s_lshl_b32 s40, s59, 7\n\t"
        "buffer_load_dwordx4 v109, %[rx], s40 offen lds\n\t"
        "s_add_u32 m0, s47, 100352\n\t"
        "s_lshl_b32 s40, s59, 7\n\t"
        "buffer_load_dwordx4 v110, %[rx], s40 offen lds\n\t"
        "ds_read_b128 v[56:59], v115 offset:38912\n\t"
        "ds_read_b128 a[40:43], v119 offset:8192\n\t"
        "ds_read_b128 a[44:47], v120 offset:8192\n\t"
        "s_waitcnt lgkmcnt(12)\n\t"
        "v_mfma_f32_16x16x32_bf16 v[40:43], a[24:27], v[48:51], v[40:43]\n\t"
        "v_mfma_f32_16x16x32_bf16 v[44:47], a[28:31], v[48:51], v[44:47]\n\t"
        "ds_read_b128 v[48:51], v115 offset:40960\n\t"
        "s_waitcnt lgkmcnt(11)\n\t"
        "s_waitcnt lgkmcnt(10)\n\t"
        "v_mfma_f32_16x16x32_bf16 v[16:19], a[32:35], v[52:55], v[16:19]\n\t"
        "v_mfma_f32_16x16x32_bf16 v[20:23], a[36:39], v[52:55], v[20:23]\n\t"
        "ds_read_b128 v[52:55], v115 offset:43008\n\t"
        "s_waitcnt lgkmcnt(4)\n\t"
        "v_mfma_f32_16x16x32_bf16 v[24:27], a[32:35], v[56:59], v[24:27]\n\t"
        "v_mfma_f32_16x16x32_bf16 v[28:31], a[36:39], v[56:59], v[28:31]\n\t"
        "ds_read_b128 v[56:59], v116 offset:36864\n\t"
        "s_waitcnt lgkmcnt(2)\n\t"
        "v_mfma_f32_16x16x32_bf16 v[32:35], a[32:35], v[48:51], v[32:35]\n\t"
        "v_mfma_f32_16x16x32_bf16 v[36:39], a[36:39], v[48:51], v[36:39]\n\t"
        "ds_read_b128 v[48:51], v116 offset:38912\n\t"
        "s_waitcnt lgkmcnt(2)\n\t"
        "v_mfma_f32_16x16x32_bf16 v[40:43], a[32:35], v[52:55], v[40:43]\n\t"
        "v_mfma_f32_16x16x32_bf16 v[44:47], a[36:39], v[52:55], v[44:47]\n\t"
        "ds_read_b128 v[52:55], v116 offset:40960\n\t"
        "s_waitcnt lgkmcnt(2)\n\t"
        "v_mfma_f32_16x16x32_bf16 v[16:19], a[40:43], v[56:59], v[16:19]\n\t"
        "v_mfma_f32_16x16x32_bf16 v[20:23], a[44:47], v[56:59], v[20:23]\n\t"
        "ds_read_b128 v[56:59], v116 offset:43008\n\t"
        "s_waitcnt vmcnt(12)\n\t"
        "s_waitcnt lgkmcnt(0)\n\t"
        "s_branch L_join3_%=\n\t"
        "L_slow4_%=:\n\t"
        "v_subrev_u32 v124, 143360, v121\n\t"
        "v_lshrrev_b32 v124, 1, v124\n\t"
        "s_mul_i32 s40, s58, 9216\n\t"
        "s_lshl_b32 s41, %[cgb], 1\n\t"
        "s_add_u32 s40, s40, s41\n\t"
        "buffer_load_dwordx2 v[92:93], v124, %[rs], s40 offen\n\t"
        "s_waitcnt vmcnt(0)\n\t"
        "v_lshlrev_b32 v100, 16, v92\n\t"
        "v_and_b32 v101, 0xffff0000, v92\n\t"
        "v_lshlrev_b32 v102, 16, v93\n\t"
        "v_and_b32 v103, 0xffff0000, v93\n\t"
        "buffer_load_dwordx2 v[92:93], v124, %[rz], s40 offen\n\t"
        "s_waitcnt vmcnt(0)\n\t"
        "v_lshlrev_b32 v104, 16, v92\n\t"
        "v_and_b32 v105, 0xffff0000, v92\n\t"
        "v_lshlrev_b32 v106, 16, v93\n\t"
        "v_and_b32 v107, 0xffff0000, v93\n\t"
        "s_mul_i32 s52, s60, 1179648\n\t"
        "v_cvt_f32_i32 v60, v60\n\t"
        "v_cvt_f32_i32 v61, v61\n\t"
        "v_cvt_f32_i32 v62, v62\n\t"
        "v_cvt_f32_i32 v63, v63\n\t"
        "v_cvt_f32_i32 v64, v64\n\t"
        "v_cvt_f32_i32 v65, v65\n\t"
        "v_cvt_f32_i32 v66, v66\n\t"
        "v_cvt_f32_i32 v67, v67\n\t"
        "v_cvt_pk_bf16_f32 v60, 0, v60\n\t"
        "v_cvt_pk_bf16_f32 v61, 0, v61\n\t"
        "v_cvt_pk_bf16_f32 v62, 0, v62\n\t"
        "v_cvt_pk_bf16_f32 v63, 0, v63\n\t"
        "v_cvt_pk_bf16_f32 v64, 0, v64\n\t"
        "v_cvt_pk_bf16_f32 v65, 0, v65\n\t"
        "v_cvt_pk_bf16_f32 v66, 0, v66\n\t"
        "v_cvt_pk_bf16_f32 v67, 0, v67\n\t"
        "v_sub_f32 v60, v60, v104\n\t"
        "v_sub_f32 v61, v61, v105\n\t"
        "v_sub_f32 v62, v62, v106\n\t"
        "v_sub_f32 v63, v63, v107\n\t"
        "v_sub_f32 v64, v64, v104\n\t"
        "v_sub_f32 v65, v65, v105\n\t"
        "v_sub_f32 v66, v66, v106\n\t"
        "v_sub_f32 v67, v67, v107\n\t"
        "v_cvt_pk_bf16_f32 v60, 0, v60\n\t"
        "v_cvt_pk_bf16_f32 v61, 0, v61\n\t"
        "v_cvt_pk_bf16_f32 v62, 0, v62\n\t"
        "v_cvt_pk_bf16_f32 v63, 0, v63\n\t"
        "v_cvt_pk_bf16_f32 v64, 0, v64\n\t"
        "v_cvt_pk_bf16_f32 v65, 0, v65\n\t"
        "v_cvt_pk_bf16_f32 v66, 0, v66\n\t"
        "v_cvt_pk_bf16_f32 v67, 0, v67\n\t"
        "v_mul_f32 v60, v60, v100\n\t"
        "v_mul_f32 v61, v61, v101\n\t"
        "v_mul_f32 v62, v62, v102\n\t"
        "v_mul_f32 v63, v63, v103\n\t"
        "v_mul_f32 v64, v64, v100\n\t"
        "v_mul_f32 v65, v65, v101\n\t"
        "v_mul_f32 v66, v66, v102\n\t"
        "v_mul_f32 v67, v67, v103\n\t"
        "v_cvt_pk_bf16_f32 v92, v60, v64\n\t"
        "v_cvt_pk_bf16_f32 v94, v61, v65\n\t"
        "v_cvt_pk_bf16_f32 v96, v62, v66\n\t"
        "v_cvt_pk_bf16_f32 v98, v63, v67\n\t"
        "buffer_load_dwordx4 v[60:63], v108, %[rq], s52 offen\n\t"
        "s_add_u32 s52, s52, 18432\n\t"
        "buffer_load_dwordx4 v[64:67], v108, %[rq], s52 offen\n\t"
        "v_cvt_f32_i32 v68, v68\n\t"
        "v_cvt_f32_i32 v69, v69\n\t"
        "v_cvt_f32_i32 v70, v70\n\t"
        "v_cvt_f32_i32 v71, v71\n\t"
        "v_cvt_f32_i32 v72, v72\n\t"
        "v_cvt_f32_i32 v73, v73\n\t"
        "v_cvt_f32_i32 v74, v74\n\t"
        "v_cvt_f32_i32 v75, v75\n\t"
        "v_cvt_pk_bf16_f32 v68, 0, v68\n\t"
        "v_cvt_pk_bf16_f32 v69, 0, v69\n\t"
        "v_cvt_pk_bf16_f32 v70, 0, v70\n\t"
        "v_cvt_pk_bf16_f32 v71, 0, v71\n\t"
        "v_cvt_pk_bf16_f32 v72, 0, v72\n\t"
        "v_cvt_pk_bf16_f32 v73, 0, v73\n\t"
        "v_cvt_pk_bf16_f32 v74, 0, v74\n\t"
        "v_cvt_pk_bf16_f32 v75, 0, v75\n\t"
        "v_sub_f32 v68, v68, v104\n\t"
        "v_sub_f32 v69, v69, v105\n\t"
        "v_sub_f32 v70, v70, v106\n\t"
        "v_sub_f32 v71, v71, v107\n\t"
        "v_sub_f32 v72, v72, v104\n\t"
        "v_sub_f32 v73, v73, v105\n\t"
        "v_sub_f32 v74, v74, v106\n\t"
        "v_sub_f32 v75, v75, v107\n\t"
        "v_cvt_pk_bf16_f32 v68, 0, v68\n\t"
        "v_cvt_pk_bf16_f32 v69, 0, v69\n\t"
        "v_cvt_pk_bf16_f32 v70, 0, v70\n\t"
        "v_cvt_pk_bf16_f32 v71, 0, v71\n\t"
        "v_cvt_pk_bf16_f32 v72, 0, v72\n\t"
        "v_cvt_pk_bf16_f32 v73, 0, v73\n\t"
        "v_cvt_pk_bf16_f32 v74, 0, v74\n\t"
        "v_cvt_pk_bf16_f32 v75, 0, v75\n\t"
        "v_mul_f32 v68, v68, v100\n\t"
        "v_mul_f32 v69, v69, v101\n\t"
        "v_mul_f32 v70, v70, v102\n\t"
        "v_mul_f32 v71, v71, v103\n\t"
        "v_mul_f32 v72, v72, v100\n\t"
        "v_mul_f32 v73, v73, v101\n\t"
        "v_mul_f32 v74, v74, v102\n\t"
        "v_mul_f32 v75, v75, v103\n\t"
        "v_cvt_pk_bf16_f32 v93, v68, v72\n\t"
        "ds_write_b64 v111, v[92:93] offset:0\n\t"
        "v_cvt_pk_bf16_f32 v95, v69, v73\n\t"
        "ds_write_b64 v112, v[94:95] offset:0\n\t"
        "v_cvt_pk_bf16_f32 v97, v70, v74\n\t"
        "ds_write_b64 v113, v[96:97] offset:0\n\t"
        "v_cvt_pk_bf16_f32 v99, v71, v75\n\t"
        "ds_write_b64 v114, v[98:99] offset:0\n\t"
        "s_add_u32 s52, s52, 18432\n\t"
        "buffer_load_dwordx4 v[68:71], v108, %[rq], s52 offen\n\t"
        "s_add_u32 s52, s52, 18432\n\t"
        "buffer_load_dwordx4 v[72:75], v108, %[rq], s52 offen\n\t"
        "s_lshl_b32 s40, s59, 9\n\t"
        "v_add_u32 v124, s40, v121\n\t"
        "ds_read_b128 v[100:103], v124\n\t"
        "ds_read_b128 v[104:107], v124 offset:4608\n\t"
        "s_add_u32 m0, s47, 0\n\t"
        "s_lshl_b32 s40, s60, 7\n\t"
        "buffer_load_dwordx4 v109, %[rx], s40 offen lds\n\t"
        "s_add_u32 m0, s47, 8192\n\t"
        "s_lshl_b32 s40, s60, 7\n\t"
        "buffer_load_dwordx4 v110, %[rx], s40 offen lds\n\t"
        "ds_read_b128 v[52:55], v117 offset:2048\n\t"
        "ds_read_b128 a[56:59], v119 offset:24576\n\t"
        "ds_read_b128 a[60:63], v120 offset:24576\n\t"
        "s_waitcnt lgkmcnt(12)\n\t"
        "v_mfma_f32_16x16x32_bf16 v[40:43], a[40:43], v[56:59], v[40:43]\n\t"
        "v_mfma_f32_16x16x32_bf16 v[44:47], a[44:47], v[56:59], v[44:47]\n\t"
        "ds_read_b128 v[56:59], v117 offset:4096\n\t"
        "s_waitcnt lgkmcnt(11)\n\t"
        "s_waitcnt lgkmcnt(10)\n\t"
        "v_mfma_f32_16x16x32_bf16 v[16:19], a[48:51], v[48:51], v[16:19]\n\t"
        "v_mfma_f32_16x16x32_bf16 v[20:23], a[52:55], v[48:51], v[20:23]\n\t"
        "ds_read_b128 v[48:51], v117 offset:6144\n\t"
        "s_waitcnt lgkmcnt(4)\n\t"
        "v_mfma_f32_16x16x32_bf16 v[24:27], a[48:51], v[52:55], v[24:27]\n\t"
        "v_mfma_f32_16x16x32_bf16 v[28:31], a[52:55], v[52:55], v[28:31]\n\t"
        "ds_read_b128 v[52:55], v118 offset:0\n\t"
        "s_waitcnt lgkmcnt(2)\n\t"
        "v_mfma_f32_16x16x32_bf16 v[32:35], a[48:51], v[56:59], v[32:35]\n\t"
        "v_mfma_f32_16x16x32_bf16 v[36:39], a[52:55], v[56:59], v[36:39]\n\t"
        "ds_read_b128 v[56:59], v118 offset:2048\n\t"
        "s_waitcnt lgkmcnt(2)\n\t"
        "v_mfma_f32_16x16x32_bf16 v[40:43], a[48:51], v[48:51], v[40:43]\n\t"
        "v_mfma_f32_16x16x32_bf16 v[44:47], a[52:55], v[48:51], v[44:47]\n\t"
        "ds_read_b128 v[48:51], v118 offset:4096\n\t"
        "s_waitcnt lgkmcnt(2)\n\t"
        "v_mfma_f32_16x16x32_bf16 v[16:19], a[56:59], v[52:55], v[16:19]\n\t"
        "v_mfma_f32_16x16x32_bf16 v[20:23], a[60:63], v[52:55], v[20:23]\n\t"
        "ds_read_b128 v[52:55], v118 offset:6144\n\t"
        "s_waitcnt vmcnt(12)\n\t"
        "s_waitcnt lgkmcnt(0)\n\t"
        "s_branch L_join4_%=\n\t"
        "L_slow5_%=:\n\t"
        "v_subrev_u32 v124, 143360, v121\n\t"
        "v_lshrrev_b32 v124, 1, v124\n\t"
        "s_mul_i32 s40, s59, 9216\n\t"
        "s_lshl_b32 s41, %[cgb], 1\n\t"
        "s_add_u32 s40, s40, s41\n\t"
        "buffer_load_dwordx2 v[92:93], v124, %[rs], s40 offen\n\t"
        "s_waitcnt vmcnt(0)\n\t"
        "v_lshlrev_b32 v100, 16, v92\n\t"
        "v_and_b32 v101, 0xffff0000, v92\n\t"
        "v_lshlrev_b32 v102, 16, v93\n\t"
        "v_and_b32 v103, 0xffff0000, v93\n\t"
        "buffer_load_dwordx2 v[92:93], v124, %[rz], s40 offen\n\t"
        "s_waitcnt vmcnt(0)\n\t"
        "v_lshlrev_b32 v104, 16, v92\n\t"
        "v_and_b32 v105, 0xffff0000, v92\n\t"
        "v_lshlrev_b32 v106, 16, v93\n\t"
        "v_and_b32 v107, 0xffff0000, v93\n\t"
        "s_mul_i32 s52, s61, 1179648\n\t"
        "v_cvt_f32_i32 v76, v76\n\t"
        "v_cvt_f32_i32 v77, v77\n\t"
        "v_cvt_f32_i32 v78, v78\n\t"
        "v_cvt_f32_i32 v79, v79\n\t"
        "v_cvt_f32_i32 v80, v80\n\t"
        "v_cvt_f32_i32 v81, v81\n\t"
        "v_cvt_f32_i32 v82, v82\n\t"
        "v_cvt_f32_i32 v83, v83\n\t"
        "v_cvt_pk_bf16_f32 v76, 0, v76\n\t"
        "v_cvt_pk_bf16_f32 v77, 0, v77\n\t"
        "v_cvt_pk_bf16_f32 v78, 0, v78\n\t"
        "v_cvt_pk_bf16_f32 v79, 0, v79\n\t"
        "v_cvt_pk_bf16_f32 v80, 0, v80\n\t"
        "v_cvt_pk_bf16_f32 v81, 0, v81\n\t"
        "v_cvt_pk_bf16_f32 v82, 0, v82\n\t"
        "v_cvt_pk_bf16_f32 v83, 0, v83\n\t"
        "v_sub_f32 v76, v76, v104\n\t"
        "v_sub_f32 v77, v77, v105\n\t"
        "v_sub_f32 v78, v78, v106\n\t"
        "v_sub_f32 v79, v79, v107\n\t"
        "v_sub_f32 v80, v80, v104\n\t"
        "v_sub_f32 v81, v81, v105\n\t"
        "v_sub_f32 v82, v82, v106\n\t"
        "v_sub_f32 v83, v83, v107\n\t"
        "v_cvt_pk_bf16_f32 v76, 0, v76\n\t"
        "v_cvt_pk_bf16_f32 v77, 0, v77\n\t"
        "v_cvt_pk_bf16_f32 v78, 0, v78\n\t"
        "v_cvt_pk_bf16_f32 v79, 0, v79\n\t"
        "v_cvt_pk_bf16_f32 v80, 0, v80\n\t"
        "v_cvt_pk_bf16_f32 v81, 0, v81\n\t"
        "v_cvt_pk_bf16_f32 v82, 0, v82\n\t"
        "v_cvt_pk_bf16_f32 v83, 0, v83\n\t"
        "v_mul_f32 v76, v76, v100\n\t"
        "v_mul_f32 v77, v77, v101\n\t"
        "v_mul_f32 v78, v78, v102\n\t"
        "v_mul_f32 v79, v79, v103\n\t"
        "v_mul_f32 v80, v80, v100\n\t"
        "v_mul_f32 v81, v81, v101\n\t"
        "v_mul_f32 v82, v82, v102\n\t"
        "v_mul_f32 v83, v83, v103\n\t"
        "v_cvt_pk_bf16_f32 v92, v76, v80\n\t"
        "v_cvt_pk_bf16_f32 v94, v77, v81\n\t"
        "v_cvt_pk_bf16_f32 v96, v78, v82\n\t"
        "v_cvt_pk_bf16_f32 v98, v79, v83\n\t"
        "buffer_load_dwordx4 v[76:79], v108, %[rq], s52 offen\n\t"
        "s_add_u32 s52, s52, 18432\n\t"
        "buffer_load_dwordx4 v[80:83], v108, %[rq], s52 offen\n\t"
        "v_cvt_f32_i32 v84, v84\n\t"
        "v_cvt_f32_i32 v85, v85\n\t"
        "v_cvt_f32_i32 v86, v86\n\t"
        "v_cvt_f32_i32 v87, v87\n\t"
        "v_cvt_f32_i32 v88, v88\n\t"
        "v_cvt_f32_i32 v89, v89\n\t"
        "v_cvt_f32_i32 v90, v90\n\t"
        "v_cvt_f32_i32 v91, v91\n\t"
        "v_cvt_pk_bf16_f32 v84, 0, v84\n\t"
        "v_cvt_pk_bf16_f32 v85, 0, v85\n\t"
        "v_cvt_pk_bf16_f32 v86, 0, v86\n\t"
        "v_cvt_pk_bf16_f32 v87, 0, v87\n\t"
        "v_cvt_pk_bf16_f32 v88, 0, v88\n\t"
        "v_cvt_pk_bf16_f32 v89, 0, v89\n\t"
        "v_cvt_pk_bf16_f32 v90, 0, v90\n\t"
        "v_cvt_pk_bf16_f32 v91, 0, v91\n\t"
        "v_sub_f32 v84, v84, v104\n\t"
        "v_sub_f32 v85, v85, v105\n\t"
        "v_sub_f32 v86, v86, v106\n\t"
        "v_sub_f32 v87, v87, v107\n\t"
        "v_sub_f32 v88, v88, v104\n\t"
        "v_sub_f32 v89, v89, v105\n\t"
        "v_sub_f32 v90, v90, v106\n\t"
        "v_sub_f32 v91, v91, v107\n\t"
        "v_cvt_pk_bf16_f32 v84, 0, v84\n\t"
        "v_cvt_pk_bf16_f32 v85, 0, v85\n\t"
        "v_cvt_pk_bf16_f32 v86, 0, v86\n\t"
        "v_cvt_pk_bf16_f32 v87, 0, v87\n\t"
        "v_cvt_pk_bf16_f32 v88, 0, v88\n\t"
        "v_cvt_pk_bf16_f32 v89, 0, v89\n\t"
        "v_cvt_pk_bf16_f32 v90, 0, v90\n\t"
        "v_cvt_pk_bf16_f32 v91, 0, v91\n\t"
        "v_mul_f32 v84, v84, v100\n\t"
        "v_mul_f32 v85, v85, v101\n\t"
        "v_mul_f32 v86, v86, v102\n\t"
        "v_mul_f32 v87, v87, v103\n\t"
        "v_mul_f32 v88, v88, v100\n\t"
        "v_mul_f32 v89, v89, v101\n\t"
        "v_mul_f32 v90, v90, v102\n\t"
        "v_mul_f32 v91, v91, v103\n\t"
        "v_cvt_pk_bf16_f32 v93, v84, v88\n\t"
        "ds_write_b64 v111, v[92:93] offset:16384\n\t"
        "v_cvt_pk_bf16_f32 v95, v85, v89\n\t"
        "ds_write_b64 v112, v[94:95] offset:16384\n\t"
        "v_cvt_pk_bf16_f32 v97, v86, v90\n\t"
        "ds_write_b64 v113, v[96:97] offset:16384\n\t"
        "v_cvt_pk_bf16_f32 v99, v87, v91\n\t"
        "ds_write_b64 v114, v[98:99] offset:16384\n\t"
        "s_add_u32 s52, s52, 18432\n\t"
        "buffer_load_dwordx4 v[84:87], v108, %[rq], s52 offen\n\t"
        "s_add_u32 s52, s52, 18432\n\t"
        "buffer_load_dwordx4 v[88:91], v108, %[rq], s52 offen\n\t"
        "s_lshl_b32 s40, s60, 9\n\t"
        "v_add_u32 v124, s40, v121\n\t"
        "ds_read_b128 v[100:103], v124\n\t"
        "ds_read_b128 v[104:107], v124 offset:4608\n\t"
        "s_add_u32 m0, s47, 18432\n\t"
        "s_lshl_b32 s40, s61, 7\n\t"
        "buffer_load_dwordx4 v109, %[rx], s40 offen lds\n\t"
        "s_add_u32 m0, s47, 26624\n\t"
        "s_lshl_b32 s40, s61, 7\n\t"
        "buffer_load_dwordx4 v110, %[rx], s40 offen lds\n\t"
        "ds_read_b128 v[48:51], v117 offset:20480\n\t"
        "ds_read_b128 a[72:75], v119 offset:8192\n\t"
        "ds_read_b128 a[76:79], v120 offset:8192\n\t"
        "s_waitcnt lgkmcnt(12)\n\t"
        "v_mfma_f32_16x16x32_bf16 v[40:43], a[56:59], v[52:55], v[40:43]\n\t"
        "v_mfma_f32_16x16x32_bf16 v[44:47], a[60:63], v[52:55], v[44:47]\n\t"
        "ds_read_b128 v[52:55], v117 offset:22528\n\t"
        "s_waitcnt lgkmcnt(11)\n\t"
        "s_waitcnt lgkmcnt(10)\n\t"
        "v_mfma_f32_16x16x32_bf16 v[16:19], a[64:67], v[56:59], v[16:19]\n\t"
        "v_mfma_f32_16x16x32_bf16 v[20:23], a[68:71], v[56:59], v[20:23]\n\t"
        "ds_read_b128 v[56:59], v117 offset:24576\n\t"
        "s_waitcnt lgkmcnt(4)\n\t"
        "v_mfma_f32_16x16x32_bf16 v[24:27], a[64:67], v[48:51], v[24:27]\n\t"
        "v_mfma_f32_16x16x32_bf16 v[28:31], a[68:71], v[48:51], v[28:31]\n\t"
        "ds_read_b128 v[48:51], v118 offset:18432\n\t"
        "s_waitcnt lgkmcnt(2)\n\t"
        "v_mfma_f32_16x16x32_bf16 v[32:35], a[64:67], v[52:55], v[32:35]\n\t"
        "v_mfma_f32_16x16x32_bf16 v[36:39], a[68:71], v[52:55], v[36:39]\n\t"
        "ds_read_b128 v[52:55], v118 offset:20480\n\t"
        "s_waitcnt lgkmcnt(2)\n\t"
        "v_mfma_f32_16x16x32_bf16 v[40:43], a[64:67], v[56:59], v[40:43]\n\t"
        "v_mfma_f32_16x16x32_bf16 v[44:47], a[68:71], v[56:59], v[44:47]\n\t"
        "ds_read_b128 v[56:59], v118 offset:22528\n\t"
        "s_waitcnt lgkmcnt(2)\n\t"
        "v_mfma_f32_16x16x32_bf16 v[16:19], a[72:75], v[48:51], v[16:19]\n\t"
        "v_mfma_f32_16x16x32_bf16 v[20:23], a[76:79], v[48:51], v[20:23]\n\t"
        "ds_read_b128 v[48:51], v118 offset:24576\n\t"
        "s_waitcnt vmcnt(12)\n\t"
        "s_waitcnt lgkmcnt(0)\n\t"
        "s_branch L_join5_%=\n\t"
        "L_slow6_%=:\n\t"
        "v_subrev_u32 v124, 143360, v121\n\t"
        "v_lshrrev_b32 v124, 1, v124\n\t"
        "s_mul_i32 s40, s60, 9216\n\t"
        "s_lshl_b32 s41, %[cgb], 1\n\t"
        "s_add_u32 s40, s40, s41\n\t"
        "buffer_load_dwordx2 v[92:93], v124, %[rs], s40 offen\n\t"
        "s_waitcnt vmcnt(0)\n\t"
        "v_lshlrev_b32 v100, 16, v92\n\t"
        "v_and_b32 v101, 0xffff0000, v92\n\t"
        "v_lshlrev_b32 v102, 16, v93\n\t"
        "v_and_b32 v103, 0xffff0000, v93\n\t"
        "buffer_load_dwordx2 v[92:93], v124, %[rz], s40 offen\n\t"
        "s_waitcnt vmcnt(0)\n\t"
        "v_lshlrev_b32 v104, 16, v92\n\t"
        "v_and_b32 v105, 0xffff0000, v92\n\t"
        "v_lshlrev_b32 v106, 16, v93\n\t"
        "v_and_b32 v107, 0xffff0000, v93\n\t"
        "s_mul_i32 s52, s62, 1179648\n\t"
        "v_cvt_f32_i32 v60, v60\n\t"
        "v_cvt_f32_i32 v61, v61\n\t"
        "v_cvt_f32_i32 v62, v62\n\t"
        "v_cvt_f32_i32 v63, v63\n\t"
        "v_cvt_f32_i32 v64, v64\n\t"
        "v_cvt_f32_i32 v65, v65\n\t"
        "v_cvt_f32_i32 v66, v66\n\t"
        "v_cvt_f32_i32 v67, v67\n\t"
        "v_cvt_pk_bf16_f32 v60, 0, v60\n\t"
        "v_cvt_pk_bf16_f32 v61, 0, v61\n\t"
        "v_cvt_pk_bf16_f32 v62, 0, v62\n\t"
        "v_cvt_pk_bf16_f32 v63, 0, v63\n\t"
        "v_cvt_pk_bf16_f32 v64, 0, v64\n\t"
        "v_cvt_pk_bf16_f32 v65, 0, v65\n\t"
        "v_cvt_pk_bf16_f32 v66, 0, v66\n\t"
        "v_cvt_pk_bf16_f32 v67, 0, v67\n\t"
        "v_sub_f32 v60, v60, v104\n\t"
        "v_sub_f32 v61, v61, v105\n\t"
        "v_sub_f32 v62, v62, v106\n\t"
        "v_sub_f32 v63, v63, v107\n\t"
        "v_sub_f32 v64, v64, v104\n\t"
        "v_sub_f32 v65, v65, v105\n\t"
        "v_sub_f32 v66, v66, v106\n\t"
        "v_sub_f32 v67, v67, v107\n\t"
        "v_cvt_pk_bf16_f32 v60, 0, v60\n\t"
        "v_cvt_pk_bf16_f32 v61, 0, v61\n\t"
        "v_cvt_pk_bf16_f32 v62, 0, v62\n\t"
        "v_cvt_pk_bf16_f32 v63, 0, v63\n\t"
        "v_cvt_pk_bf16_f32 v64, 0, v64\n\t"
        "v_cvt_pk_bf16_f32 v65, 0, v65\n\t"
        "v_cvt_pk_bf16_f32 v66, 0, v66\n\t"
        "v_cvt_pk_bf16_f32 v67, 0, v67\n\t"
        "v_mul_f32 v60, v60, v100\n\t"
        "v_mul_f32 v61, v61, v101\n\t"
        "v_mul_f32 v62, v62, v102\n\t"
        "v_mul_f32 v63, v63, v103\n\t"
        "v_mul_f32 v64, v64, v100\n\t"
        "v_mul_f32 v65, v65, v101\n\t"
        "v_mul_f32 v66, v66, v102\n\t"
        "v_mul_f32 v67, v67, v103\n\t"
        "v_cvt_pk_bf16_f32 v92, v60, v64\n\t"
        "v_cvt_pk_bf16_f32 v94, v61, v65\n\t"
        "v_cvt_pk_bf16_f32 v96, v62, v66\n\t"
        "v_cvt_pk_bf16_f32 v98, v63, v67\n\t"
        "buffer_load_dwordx4 v[60:63], v108, %[rq], s52 offen\n\t"
        "s_add_u32 s52, s52, 18432\n\t"
        "buffer_load_dwordx4 v[64:67], v108, %[rq], s52 offen\n\t"
        "v_cvt_f32_i32 v68, v68\n\t"
        "v_cvt_f32_i32 v69, v69\n\t"
        "v_cvt_f32_i32 v70, v70\n\t"
        "v_cvt_f32_i32 v71, v71\n\t"
        "v_cvt_f32_i32 v72, v72\n\t"
        "v_cvt_f32_i32 v73, v73\n\t"
        "v_cvt_f32_i32 v74, v74\n\t"
        "v_cvt_f32_i32 v75, v75\n\t"
        "v_cvt_pk_bf16_f32 v68, 0, v68\n\t"
        "v_cvt_pk_bf16_f32 v69, 0, v69\n\t"
        "v_cvt_pk_bf16_f32 v70, 0, v70\n\t"
        "v_cvt_pk_bf16_f32 v71, 0, v71\n\t"
        "v_cvt_pk_bf16_f32 v72, 0, v72\n\t"
        "v_cvt_pk_bf16_f32 v73, 0, v73\n\t"
        "v_cvt_pk_bf16_f32 v74, 0, v74\n\t"
        "v_cvt_pk_bf16_f32 v75, 0, v75\n\t"
        "v_sub_f32 v68, v68, v104\n\t"
        "v_sub_f32 v69, v69, v105\n\t"
        "v_sub_f32 v70, v70, v106\n\t"
        "v_sub_f32 v71, v71, v107\n\t"
        "v_sub_f32 v72, v72, v104\n\t"
        "v_sub_f32 v73, v73, v105\n\t"
        "v_sub_f32 v74, v74, v106\n\t"
        "v_sub_f32 v75, v75, v107\n\t"
        "v_cvt_pk_bf16_f32 v68, 0, v68\n\t"
        "v_cvt_pk_bf16_f32 v69, 0, v69\n\t"
        "v_cvt_pk_bf16_f32 v70, 0, v70\n\t"
        "v_cvt_pk_bf16_f32 v71, 0, v71\n\t"
        "v_cvt_pk_bf16_f32 v72, 0, v72\n\t"
        "v_cvt_pk_bf16_f32 v73, 0, v73\n\t"
        "v_cvt_pk_bf16_f32 v74, 0, v74\n\t"
        "v_cvt_pk_bf16_f32 v75, 0, v75\n\t"
        "v_mul_f32 v68, v68, v100\n\t"
        "v_mul_f32 v69, v69, v101\n\t"
        "v_mul_f32 v70, v70, v102\n\t"
        "v_mul_f32 v71, v71, v103\n\t"
        "v_mul_f32 v72, v72, v100\n\t"
        "v_mul_f32 v73, v73, v101\n\t"
        "v_mul_f32 v74, v74, v102\n\t"
        "v_mul_f32 v75, v75, v103\n\t"
        "v_cvt_pk_bf16_f32 v93, v68, v72\n\t"
        "ds_write_b64 v111, v[92:93] offset:0\n\t"
        "v_cvt_pk_bf16_f32 v95, v69, v73\n\t"
        "ds_write_b64 v112, v[94:95] offset:0\n\t"
        "v_cvt_pk_bf16_f32 v97, v70, v74\n\t"
        "ds_write_b64 v113, v[96:97] offset:0\n\t"
        "v_cvt_pk_bf16_f32 v99, v71, v75\n\t"
        "ds_write_b64 v114, v[98:99] offset:0\n\t"
        "s_add_u32 s52, s52, 18432\n\t"
        "buffer_load_dwordx4 v[68:71], v108, %[rq], s52 offen\n\t"
        "s_add_u32 s52, s52, 18432\n\t"
        "buffer_load_dwordx4 v[72:75], v108, %[rq], s52 offen\n\t"
        "s_lshl_b32 s40, s61, 9\n\t"
        "v_add_u32 v124, s40, v121\n\t"
        "ds_read_b128 v[100:103], v124\n\t"
        "ds_read_b128 v[104:107], v124 offset:4608\n\t"
        "s_add_u32 m0, s47, 36864\n\t"
        "s_lshl_b32 s40, s62, 7\n\t"
        "buffer_load_dwordx4 v109, %[rx], s40 offen lds\n\t"
        "s_add_u32 m0, s47, 45056\n\t"
        "s_lshl_b32 s40, s62, 7\n\t"
        "buffer_load_dwordx4 v110, %[rx], s40 offen lds\n\t"
        "ds_read_b128 v[56:59], v117 offset:38912\n\t"
        "ds_read_b128 a[88:91], v119 offset:24576\n\t"
        "ds_read_b128 a[92:95], v120 offset:24576\n\t"
        "s_waitcnt lgkmcnt(12)\n\t"
        "v_mfma_f32_16x16x32_bf16 v[40:43], a[72:75], v[48:51], v[40:43]\n\t"
        "v_mfma_f32_16x16x32_bf16 v[44:47], a[76:79], v[48:51], v[44:47]\n\t"
        "ds_read_b128 v[48:51], v117 offset:40960\n\t"
        "s_waitcnt lgkmcnt(11)\n\t"
        "s_waitcnt lgkmcnt(10)\n\t"
        "v_mfma_f32_16x16x32_bf16 v[16:19], a[80:83], v[52:55], v[16:19]\n\t"
        "v_mfma_f32_16x16x32_bf16 v[20:23], a[84:87], v[52:55], v[20:23]\n\t"
        "ds_read_b128 v[52:55], v117 offset:43008\n\t"
        "s_waitcnt lgkmcnt(4)\n\t"
        "v_mfma_f32_16x16x32_bf16 v[24:27], a[80:83], v[56:59], v[24:27]\n\t"
        "v_mfma_f32_16x16x32_bf16 v[28:31], a[84:87], v[56:59], v[28:31]\n\t"
        "ds_read_b128 v[56:59], v118 offset:36864\n\t"
        "s_waitcnt lgkmcnt(2)\n\t"
        "v_mfma_f32_16x16x32_bf16 v[32:35], a[80:83], v[48:51], v[32:35]\n\t"
        "v_mfma_f32_16x16x32_bf16 v[36:39], a[84:87], v[48:51], v[36:39]\n\t"
        "ds_read_b128 v[48:51], v118 offset:38912\n\t"
        "s_waitcnt lgkmcnt(2)\n\t"
        "v_mfma_f32_16x16x32_bf16 v[40:43], a[80:83], v[52:55], v[40:43]\n\t"
        "v_mfma_f32_16x16x32_bf16 v[44:47], a[84:87], v[52:55], v[44:47]\n\t"
        "ds_read_b128 v[52:55], v118 offset:40960\n\t"
        "s_waitcnt lgkmcnt(2)\n\t"
        "v_mfma_f32_16x16x32_bf16 v[16:19], a[88:91], v[56:59], v[16:19]\n\t"
        "v_mfma_f32_16x16x32_bf16 v[20:23], a[92:95], v[56:59], v[20:23]\n\t"
        "ds_read_b128 v[56:59], v118 offset:43008\n\t"
        "s_waitcnt vmcnt(12)\n\t"
        "s_waitcnt lgkmcnt(0)\n\t"
        "s_branch L_join6_%=\n\t"
        "L_slow7_%=:\n\t"
        "v_subrev_u32 v124, 143360, v121\n\t"
        "v_lshrrev_b32 v124, 1, v124\n\t"
        "s_mul_i32 s40, s61, 9216\n\t"
        "s_lshl_b32 s41, %[cgb], 1\n\t"
        "s_add_u32 s40, s40, s41\n\t"
        "buffer_load_dwordx2 v[92:93], v124, %[rs], s40 offen\n\t"
        "s_waitcnt vmcnt(0)\n\t"
        "v_lshlrev_b32 v100, 16, v92\n\t"
        "v_and_b32 v101, 0xffff0000, v92\n\t"
        "v_lshlrev_b32 v102, 16, v93\n\t"
        "v_and_b32 v103, 0xffff0000, v93\n\t"
        "buffer_load_dwordx2 v[92:93], v124, %[rz], s40 offen\n\t"
        "s_waitcnt vmcnt(0)\n\t"
        "v_lshlrev_b32 v104, 16, v92\n\t"
        "v_and_b32 v105, 0xffff0000, v92\n\t"
        "v_lshlrev_b32 v106, 16, v93\n\t"
        "v_and_b32 v107, 0xffff0000, v93\n\t"
        "v_cvt_f32_i32 v76, v76\n\t"
        "v_cvt_f32_i32 v77, v77\n\t"
        "v_cvt_f32_i32 v78, v78\n\t"
        "v_cvt_f32_i32 v79, v79\n\t"
        "v_cvt_f32_i32 v80, v80\n\t"
        "v_cvt_f32_i32 v81, v81\n\t"
        "v_cvt_f32_i32 v82, v82\n\t"
        "v_cvt_f32_i32 v83, v83\n\t"
        "v_cvt_pk_bf16_f32 v76, 0, v76\n\t"
        "v_cvt_pk_bf16_f32 v77, 0, v77\n\t"
        "v_cvt_pk_bf16_f32 v78, 0, v78\n\t"
        "v_cvt_pk_bf16_f32 v79, 0, v79\n\t"
        "v_cvt_pk_bf16_f32 v80, 0, v80\n\t"
        "v_cvt_pk_bf16_f32 v81, 0, v81\n\t"
        "v_cvt_pk_bf16_f32 v82, 0, v82\n\t"
        "v_cvt_pk_bf16_f32 v83, 0, v83\n\t"
        "v_sub_f32 v76, v76, v104\n\t"
        "v_sub_f32 v77, v77, v105\n\t"
        "v_sub_f32 v78, v78, v106\n\t"
        "v_sub_f32 v79, v79, v107\n\t"
        "v_sub_f32 v80, v80, v104\n\t"
        "v_sub_f32 v81, v81, v105\n\t"
        "v_sub_f32 v82, v82, v106\n\t"
        "v_sub_f32 v83, v83, v107\n\t"
        "v_cvt_pk_bf16_f32 v76, 0, v76\n\t"
        "v_cvt_pk_bf16_f32 v77, 0, v77\n\t"
        "v_cvt_pk_bf16_f32 v78, 0, v78\n\t"
        "v_cvt_pk_bf16_f32 v79, 0, v79\n\t"
        "v_cvt_pk_bf16_f32 v80, 0, v80\n\t"
        "v_cvt_pk_bf16_f32 v81, 0, v81\n\t"
        "v_cvt_pk_bf16_f32 v82, 0, v82\n\t"
        "v_cvt_pk_bf16_f32 v83, 0, v83\n\t"
        "v_mul_f32 v76, v76, v100\n\t"
        "v_mul_f32 v77, v77, v101\n\t"
        "v_mul_f32 v78, v78, v102\n\t"
        "v_mul_f32 v79, v79, v103\n\t"
        "v_mul_f32 v80, v80, v100\n\t"
        "v_mul_f32 v81, v81, v101\n\t"
        "v_mul_f32 v82, v82, v102\n\t"
        "v_mul_f32 v83, v83, v103\n\t"
        "v_cvt_pk_bf16_f32 v92, v76, v80\n\t"
        "v_cvt_pk_bf16_f32 v94, v77, v81\n\t"
        "v_cvt_pk_bf16_f32 v96, v78, v82\n\t"
        "v_cvt_pk_bf16_f32 v98, v79, v83\n\t"
        "v_cvt_f32_i32 v84, v84\n\t"
        "v_cvt_f32_i32 v85, v85\n\t"
        "v_cvt_f32_i32 v86, v86\n\t"
        "v_cvt_f32_i32 v87, v87\n\t"
        "v_cvt_f32_i32 v88, v88\n\t"
        "v_cvt_f32_i32 v89, v89\n\t"
        "v_cvt_f32_i32 v90, v90\n\t"
        "v_cvt_f32_i32 v91, v91\n\t"
        "v_cvt_pk_bf16_f32 v84, 0, v84\n\t"
        "v_cvt_pk_bf16_f32 v85, 0, v85\n\t"
        "v_cvt_pk_bf16_f32 v86, 0, v86\n\t"
        "v_cvt_pk_bf16_f32 v87, 0, v87\n\t"
        "v_cvt_pk_bf16_f32 v88, 0, v88\n\t"
        "v_cvt_pk_bf16_f32 v89, 0, v89\n\t"
        "v_cvt_pk_bf16_f32 v90, 0, v90\n\t"
        "v_cvt_pk_bf16_f32 v91, 0, v91\n\t"
        "v_sub_f32 v84, v84, v104\n\t"
        "v_sub_f32 v85, v85, v105\n\t"
        "v_sub_f32 v86, v86, v106\n\t"
        "v_sub_f32 v87, v87, v107\n\t"
        "v_sub_f32 v88, v88, v104\n\t"
        "v_sub_f32 v89, v89, v105\n\t"
        "v_sub_f32 v90, v90, v106\n\t"
        "v_sub_f32 v91, v91, v107\n\t"
        "v_cvt_pk_bf16_f32 v84, 0, v84\n\t"
        "v_cvt_pk_bf16_f32 v85, 0, v85\n\t"
        "v_cvt_pk_bf16_f32 v86, 0, v86\n\t"
        "v_cvt_pk_bf16_f32 v87, 0, v87\n\t"
        "v_cvt_pk_bf16_f32 v88, 0, v88\n\t"
        "v_cvt_pk_bf16_f32 v89, 0, v89\n\t"
        "v_cvt_pk_bf16_f32 v90, 0, v90\n\t"
        "v_cvt_pk_bf16_f32 v91, 0, v91\n\t"
        "v_mul_f32 v84, v84, v100\n\t"
        "v_mul_f32 v85, v85, v101\n\t"
        "v_mul_f32 v86, v86, v102\n\t"
        "v_mul_f32 v87, v87, v103\n\t"
        "v_mul_f32 v88, v88, v100\n\t"
        "v_mul_f32 v89, v89, v101\n\t"
        "v_mul_f32 v90, v90, v102\n\t"
        "v_mul_f32 v91, v91, v103\n\t"
        "v_cvt_pk_bf16_f32 v93, v84, v88\n\t"
        "ds_write_b64 v111, v[92:93] offset:16384\n\t"
        "v_cvt_pk_bf16_f32 v95, v85, v89\n\t"
        "ds_write_b64 v112, v[94:95] offset:16384\n\t"
        "v_cvt_pk_bf16_f32 v97, v86, v90\n\t"
        "ds_write_b64 v113, v[96:97] offset:16384\n\t"
        "v_cvt_pk_bf16_f32 v99, v87, v91\n\t"
        "ds_write_b64 v114, v[98:99] offset:16384\n\t"
        "s_lshl_b32 s40, s62, 9\n\t"
        "v_add_u32 v124, s40, v121\n\t"
        "ds_read_b128 v[100:103], v124\n\t"
        "ds_read_b128 v[104:107], v124 offset:4608\n\t"
        "v_accvgpr_read_b32 v109, a127\n\t"
        "s_add_u32 s40, s45, 8\n\t"
        "s_add_u32 s51, %[start], s40\n\t"
        "s_cmp_ge_u32 s51, %[nblk]\n\t"
        "s_cselect_b32 s43, %[nblk], 0\n\t"
        "s_sub_u32 s51, s51, s43\n\t"
        "s_add_u32 s51, s51, %[blk0]\n\t"
        "s_mul_i32 s51, s51, 18432\n\t"
        "s_mov_b32 s41, 55296\n\t"
        "s_cmp_lg_u32 s45, 0\n\t"
        "s_cselect_b32 s41, 73728, s41\n\t"
        "s_add_u32 s50, s41, s53\n\t"
        "s_cmp_ge_u32 s44, 2\n\t"
        "s_cbranch_scc1 L_x2s2_%=\n\t"
        "s_add_u32 m0, s50, 16384\n\t"
        "s_lshl_b32 s40, s62, 7\n\t"
        "s_add_u32 s40, s40, s51\n\t"
        "buffer_load_dwordx4 v109, %[rx], s40 offen lds\n\t"
        "L_x2s2_%=:\n\t"
        "s_add_u32 m0, s50, 0\n\t"
        "s_cmp_ge_u32 s44, 2\n\t"
        "s_cselect_b32 s40, s55, s54\n\t"
        "s_lshl_b32 s40, s40, 7\n\t"
        "s_add_u32 s40, s40, s51\n\t"
        "buffer_load_dwordx4 v109, %[rx], s40 offen lds\n\t"
        "s_add_u32 m0, s50, 4096\n\t"
        "s_cmp_ge_u32 s44, 2\n\t"
        "s_cselect_b32 s40, s57, s56\n\t"
        "s_lshl_b32 s40, s40, 7\n\t"
        "s_add_u32 s40, s40, s51\n\t"
        "buffer_load_dwordx4 v109, %[rx], s40 offen lds\n\t"
        "s_add_u32 m0, s50, 8192\n\t"
        "s_cmp_ge_u32 s44, 2\n\t"
        "s_cselect_b32 s40, s59, s58\n\t"
        "s_lshl_b32 s40, s40, 7\n\t"
        "s_add_u32 s40, s40, s51\n\t"
        "buffer_load_dwordx4 v109, %[rx], s40 offen lds\n\t"
        "s_add_u32 m0, s50, 12288\n\t"
        "s_cmp_ge_u32 s44, 2\n\t"
        "s_cselect_b32 s40, s61, s60\n\t"
        "s_lshl_b32 s40, s40, 7\n\t"
        "s_add_u32 s40, s40, s51\n\t"
        "buffer_load_dwordx4 v109, %[rx], s40 offen lds\n\t"
        "ds_read_b128 v[52:55], v115 offset:2048\n\t"
        "ds_read_b128 a[104:107], v119 offset:8192\n\t"
        "ds_read_b128 a[108:111], v120 offset:8192\n\t"
        "s_waitcnt lgkmcnt(12)\n\t"
        "v_mfma_f32_16x16x32_bf16 v[40:43], a[88:91], v[56:59], v[40:43]\n\t"
        "v_mfma_f32_16x16x32_bf16 v[44:47], a[92:95], v[56:59], v[44:47]\n\t"
        "ds_read_b128 v[56:59], v115 offset:4096\n\t"
        "s_waitcnt lgkmcnt(11)\n\t"
        "s_waitcnt lgkmcnt(10)\n\t"
        "v_mfma_f32_16x16x32_bf16 v[16:19], a[96:99], v[48:51], v[16:19]\n\t"
        "v_mfma_f32_16x16x32_bf16 v[20:23], a[100:103], v[48:51], v[20:23]\n\t"
        "ds_read_b128 v[48:51], v115 offset:6144\n\t"
        "s_waitcnt lgkmcnt(4)\n\t"
        "v_mfma_f32_16x16x32_bf16 v[24:27], a[96:99], v[52:55], v[24:27]\n\t"
        "v_mfma_f32_16x16x32_bf16 v[28:31], a[100:103], v[52:55], v[28:31]\n\t"
        "ds_read_b128 v[52:55], v116 offset:0\n\t"
        "s_waitcnt lgkmcnt(2)\n\t"
        "v_mfma_f32_16x16x32_bf16 v[32:35], a[96:99], v[56:59], v[32:35]\n\t"
        "v_mfma_f32_16x16x32_bf16 v[36:39], a[100:103], v[56:59], v[36:39]\n\t"
        "ds_read_b128 v[56:59], v116 offset:2048\n\t"
        "s_waitcnt lgkmcnt(2)\n\t"
        "v_mfma_f32_16x16x32_bf16 v[40:43], a[96:99], v[48:51], v[40:43]\n\t"
        "v_mfma_f32_16x16x32_bf16 v[44:47], a[100:103], v[48:51], v[44:47]\n\t"
        "ds_read_b128 v[48:51], v116 offset:4096\n\t"
        "s_waitcnt lgkmcnt(2)\n\t"
        "v_mfma_f32_16x16x32_bf16 v[16:19], a[104:107], v[52:55], v[16:19]\n\t"
        "v_mfma_f32_16x16x32_bf16 v[20:23], a[108:111], v[52:55], v[20:23]\n\t"
        "ds_read_b128 v[52:55], v116 offset:6144\n\t"
        "s_waitcnt vmcnt(10)\n\t"
        "s_waitcnt lgkmcnt(0)\n\t"
        "s_branch L_join7_%=\n\t"
        "L_slow8_%=:\n\t"
        "v_subrev_u32 v124, 143360, v121\n\t"
        "v_lshrrev_b32 v124, 1, v124\n\t"
        "s_mul_i32 s40, s62, 9216\n\t"
        "s_lshl_b32 s41, %[cgb], 1\n\t"
        "s_add_u32 s40, s40, s41\n\t"
        "buffer_load_dwordx2 v[92:93], v124, %[rs], s40 offen\n\t"
        "s_waitcnt vmcnt(0)\n\t"
        "v_lshlrev_b32 v100, 16, v92\n\t"
        "v_and_b32 v101, 0xffff0000, v92\n\t"
        "v_lshlrev_b32 v102, 16, v93\n\t"
        "v_and_b32 v103, 0xffff0000, v93\n\t"
        "buffer_load_dwordx2 v[92:93], v124, %[rz], s40 offen\n\t"
        "s_waitcnt vmcnt(0)\n\t"
        "v_lshlrev_b32 v104, 16, v92\n\t"
        "v_and_b32 v105, 0xffff0000, v92\n\t"
        "v_lshlrev_b32 v106, 16, v93\n\t"
        "v_and_b32 v107, 0xffff0000, v93\n\t"
        "v_cvt_f32_i32 v60, v60\n\t"
        "v_cvt_f32_i32 v61, v61\n\t"
        "v_cvt_f32_i32 v62, v62\n\t"
        "v_cvt_f32_i32 v63, v63\n\t"
        "v_cvt_f32_i32 v64, v64\n\t"
        "v_cvt_f32_i32 v65, v65\n\t"
        "v_cvt_f32_i32 v66, v66\n\t"
        "v_cvt_f32_i32 v67, v67\n\t"
        "v_cvt_pk_bf16_f32 v60, 0, v60\n\t"
        "v_cvt_pk_bf16_f32 v61, 0, v61\n\t"
        "v_cvt_pk_bf16_f32 v62, 0, v62\n\t"
        "v_cvt_pk_bf16_f32 v63, 0, v63\n\t"
        "v_cvt_pk_bf16_f32 v64, 0, v64\n\t"
        "v_cvt_pk_bf16_f32 v65, 0, v65\n\t"
        "v_cvt_pk_bf16_f32 v66, 0, v66\n\t"
        "v_cvt_pk_bf16_f32 v67, 0, v67\n\t"
        "v_sub_f32 v60, v60, v104\n\t"
        "v_sub_f32 v61, v61, v105\n\t"
        "v_sub_f32 v62, v62, v106\n\t"
        "v_sub_f32 v63, v63, v107\n\t"
        "v_sub_f32 v64, v64, v104\n\t"
        "v_sub_f32 v65, v65, v105\n\t"
        "v_sub_f32 v66, v66, v106\n\t"
        "v_sub_f32 v67, v67, v107\n\t"
        "v_cvt_pk_bf16_f32 v60, 0, v60\n\t"
        "v_cvt_pk_bf16_f32 v61, 0, v61\n\t"
        "v_cvt_pk_bf16_f32 v62, 0, v62\n\t"
        "v_cvt_pk_bf16_f32 v63, 0, v63\n\t"
        "v_cvt_pk_bf16_f32 v64, 0, v64\n\t"
        "v_cvt_pk_bf16_f32 v65, 0, v65\n\t"
        "v_cvt_pk_bf16_f32 v66, 0, v66\n\t"
        "v_cvt_pk_bf16_f32 v67, 0, v67\n\t"
        "v_mul_f32 v60, v60, v100\n\t"
        "v_mul_f32 v61, v61, v101\n\t"
        "v_mul_f32 v62, v62, v102\n\t"
        "v_mul_f32 v63, v63, v103\n\t"
        "v_mul_f32 v64, v64, v100\n\t"
        "v_mul_f32 v65, v65, v101\n\t"
        "v_mul_f32 v66, v66, v102\n\t"
        "v_mul_f32 v67, v67, v103\n\t"
        "v_cvt_pk_bf16_f32 v92, v60, v64\n\t"
        "v_cvt_pk_bf16_f32 v94, v61, v65\n\t"
        "v_cvt_pk_bf16_f32 v96, v62, v66\n\t"
        "v_cvt_pk_bf16_f32 v98, v63, v67\n\t"
        "v_cvt_f32_i32 v68, v68\n\t"
        "v_cvt_f32_i32 v69, v69\n\t"
        "v_cvt_f32_i32 v70, v70\n\t"
        "v_cvt_f32_i32 v71, v71\n\t"
        "v_cvt_f32_i32 v72, v72\n\t"
        "v_cvt_f32_i32 v73, v73\n\t"
        "v_cvt_f32_i32 v74, v74\n\t"
        "v_cvt_f32_i32 v75, v75\n\t"
        "v_cvt_pk_bf16_f32 v68, 0, v68\n\t"
        "v_cvt_pk_bf16_f32 v69, 0, v69\n\t"
        "v_cvt_pk_bf16_f32 v70, 0, v70\n\t"
        "v_cvt_pk_bf16_f32 v71, 0, v71\n\t"
        "v_cvt_pk_bf16_f32 v72, 0, v72\n\t"
        "v_cvt_pk_bf16_f32 v73, 0, v73\n\t"
        "v_cvt_pk_bf16_f32 v74, 0, v74\n\t"
        "v_cvt_pk_bf16_f32 v75, 0, v75\n\t"
        "v_sub_f32 v68, v68, v104\n\t"
        "v_sub_f32 v69, v69, v105\n\t"
        "v_sub_f32 v70, v70, v106\n\t"
        "v_sub_f32 v71, v71, v107\n\t"
        "v_sub_f32 v72, v72, v104\n\t"
        "v_sub_f32 v73, v73, v105\n\t"
        "v_sub_f32 v74, v74, v106\n\t"
        "v_sub_f32 v75, v75, v107\n\t"
        "v_cvt_pk_bf16_f32 v68, 0, v68\n\t"
        "v_cvt_pk_bf16_f32 v69, 0, v69\n\t"
        "v_cvt_pk_bf16_f32 v70, 0, v70\n\t"
        "v_cvt_pk_bf16_f32 v71, 0, v71\n\t"
        "v_cvt_pk_bf16_f32 v72, 0, v72\n\t"
        "v_cvt_pk_bf16_f32 v73, 0, v73\n\t"
        "v_cvt_pk_bf16_f32 v74, 0, v74\n\t"
        "v_cvt_pk_bf16_f32 v75, 0, v75\n\t"
        "v_mul_f32 v68, v68, v100\n\t"
        "v_mul_f32 v69, v69, v101\n\t"
        "v_mul_f32 v70, v70, v102\n\t"
        "v_mul_f32 v71, v71, v103\n\t"
        "v_mul_f32 v72, v72, v100\n\t"
        "v_mul_f32 v73, v73, v101\n\t"
        "v_mul_f32 v74, v74, v102\n\t"
        "v_mul_f32 v75, v75, v103\n\t"
        "v_cvt_pk_bf16_f32 v93, v68, v72\n\t"
        "ds_write_b64 v111, v[92:93] offset:0\n\t"
        "v_cvt_pk_bf16_f32 v95, v69, v73\n\t"
        "ds_write_b64 v112, v[94:95] offset:0\n\t"
        "v_cvt_pk_bf16_f32 v97, v70, v74\n\t"
        "ds_write_b64 v113, v[96:97] offset:0\n\t"
        "v_cvt_pk_bf16_f32 v99, v71, v75\n\t"
        "ds_write_b64 v114, v[98:99] offset:0\n\t"
        "ds_read_b128 v[48:51], v115 offset:20480\n\t"
        "ds_read_b128 a[120:123], v119 offset:24576\n\t"
        "ds_read_b128 a[124:127], v120 offset:24576\n\t"
        "s_waitcnt lgkmcnt(10)\n\t"
        "v_mfma_f32_16x16x32_bf16 v[40:43], a[104:107], v[52:55], v[40:43]\n\t"
        "v_mfma_f32_16x16x32_bf16 v[44:47], a[108:111], v[52:55], v[44:47]\n\t"
        "ds_read_b128 v[52:55], v115 offset:22528\n\t"
        "s_waitcnt lgkmcnt(9)\n\t"
        "s_waitcnt lgkmcnt(8)\n\t"
        "v_mfma_f32_16x16x32_bf16 v[16:19], a[112:115], v[56:59], v[16:19]\n\t"
        "v_mfma_f32_16x16x32_bf16 v[20:23], a[116:119], v[56:59], v[20:23]\n\t"
        "ds_read_b128 v[56:59], v115 offset:24576\n\t"
        "s_waitcnt lgkmcnt(4)\n\t"
        "v_mfma_f32_16x16x32_bf16 v[24:27], a[112:115], v[48:51], v[24:27]\n\t"
        "v_mfma_f32_16x16x32_bf16 v[28:31], a[116:119], v[48:51], v[28:31]\n\t"
        "ds_read_b128 v[48:51], v116 offset:18432\n\t"
        "s_waitcnt lgkmcnt(2)\n\t"
        "v_mfma_f32_16x16x32_bf16 v[32:35], a[112:115], v[52:55], v[32:35]\n\t"
        "v_mfma_f32_16x16x32_bf16 v[36:39], a[116:119], v[52:55], v[36:39]\n\t"
        "ds_read_b128 v[52:55], v116 offset:20480\n\t"
        "s_waitcnt lgkmcnt(2)\n\t"
        "v_mfma_f32_16x16x32_bf16 v[40:43], a[112:115], v[56:59], v[40:43]\n\t"
        "v_mfma_f32_16x16x32_bf16 v[44:47], a[116:119], v[56:59], v[44:47]\n\t"
        "ds_read_b128 v[56:59], v116 offset:22528\n\t"
        "s_waitcnt lgkmcnt(2)\n\t"
        "v_mfma_f32_16x16x32_bf16 v[16:19], a[120:123], v[48:51], v[16:19]\n\t"
        "v_mfma_f32_16x16x32_bf16 v[20:23], a[124:127], v[48:51], v[20:23]\n\t"
        "ds_read_b128 v[48:51], v116 offset:24576\n\t"
        "s_waitcnt vmcnt(4)\n\t"
        "s_waitcnt lgkmcnt(0)\n\t"
        "s_branch L_join8_%=\n\t"
        "L_end_%=:\n\t"
        :
        : [rx] "s"(rx), [rq] "s"(rq), [rs] "s"(rs), [rz] "s"(rz), [ro] "s"(ro),
          [wave] "s"(wave), [cgb] "s"(cgb), [blk0] "s"(blk0), [nblk] "s"(nblk), [rot] "s"(rot), [start] "s"(start),
          [wg] "s"(wg), [lane] "v"(lane)
        : "memory", "scc", "vcc", "v0","v1","v2","v3","v4","v5","v6","v7","v8","v9","v10","v11","v12","v13","v14","v15","v16","v17","v18","v19","v20","v21","v22","v23","v24","v25","v26","v27","v28","v29","v30","v31","v32","v33","v34","v35","v36","v37","v38","v39","v40","v41","v42","v43","v44","v45","v46","v47","v48","v49","v50","v51","v52","v53","v54","v55","v56","v57","v58","v59","v60","v61","v62","v63","v64","v65","v66","v67","v68","v69","v70","v71","v72","v73","v74","v75","v76","v77","v78","v79","v80","v81","v82","v83","v84","v85","v86","v87","v88","v89","v90","v91","v92","v93","v94","v95","v96","v97","v98","v99","v100","v101","v102","v103","v104","v105","v106","v107","v108","v109","v110","v111","v112","v113","v114","v115","v116","v117","v118","v119","v120","v121","v122","v123","v124","v125", "a0","a1","a2","a3","a4","a5","a6","a7","a8","a9","a10","a11","a12","a13","a14","a15","a16","a17","a18","a19","a20","a21","a22","a23","a24","a25","a26","a27","a28","a29","a30","a31","a32","a33","a34","a35","a36","a37","a38","a39","a40","a41","a42","a43","a44","a45","a46","a47","a48","a49","a50","a51","a52","a53","a54","a55","a56","a57","a58","a59","a60","a61","a62","a63","a64","a65","a66","a67","a68","a69","a70","a71","a72","a73","a74","a75","a76","a77","a78","a79","a80","a81","a82","a83","a84","a85","a86","a87","a88","a89","a90","a91","a92","a93","a94","a95","a96","a97","a98","a99","a100","a101","a102","a103","a104","a105","a106","a107","a108","a109","a110","a111","a112","a113","a114","a115","a116","a117","a118","a119","a120","a121","a122","a123","a124","a125","a126","a127", "s40","s41","s42","s43","s44","s45","s46","s47","s48","s49","s50","s51","s52","s53","s54","s55","s56","s57","s58","s59","s60","s61","s62","s63","s64","s65","s66","s67","s68","s69","s70","s71","s72","s73","s74","s75","s76","s77","s78","s79","s80","s81","s82","s83","s84","s85","s86","s87","s88","s89","s90","s91","s92","s93","s94","s95","s96","s97","s98","s99");
}

extern "C" void kernel_launch(void* const* d_in, const int* in_sizes, int n_in,
                              void* d_out, int out_size, void* d_ws, size_t ws_size,
                              hipStream_t stream) {
    (void)hipFuncSetAttribute((const void*)wq_gemm, hipFuncAttributeMaxDynamicSharedMemorySize, LDS_BYTES);
    wq_gemm<<<252, 512, LDS_BYTES, stream>>>((const unsigned short*)d_in[0], (const int*)d_in[1],
        (const unsigned short*)d_in[2], (const unsigned short*)d_in[3], (unsigned short*)d_out);
}
```
